```python
import torch

def _softmax(x, axis=-1):
    return torch.softmax(x, dim=axis)
def _gelu(x):
    return torch.nn.functional.gelu(x, approximate="tanh")
def _silu(x):
    return torch.nn.functional.silu(x)
def _sigmoid(x):
    return torch.sigmoid(x)
def _layernorm(x, axis=-1, eps=1e-5):
    m = x.mean(dim=axis, keepdim=True)
    v = x.var(dim=axis, keepdim=True, unbiased=False)
    return (x - m) / torch.sqrt(v + eps)
def _scatter_add(tgt, idx, src, axis=0):
    return tgt.clone().index_add_(axis, idx, src)
def _segment_sum(src, seg, n):
    out = torch.zeros((n,) + src.shape[1:], dtype=src.dtype)
    return out.index_add_(0, seg, src)
def _pad(x, pw):
    flat = tuple(v for pair in reversed(pw) for v in pair)
    return torch.nn.functional.pad(x, flat)

kernel_name = 'recurrent_scan__linear_kernel_hedgehog__dense_activation_squared_relu__prompt'

def setup_inputs():

    _w = (lambda a: (lambda t: t.to(torch.bfloat16) if t.is_floating_point() else t)(torch.as_tensor(a)))
    def _A_setup():
        B, S, D, H = 1, 2048, 448, 16
        Dh = D // H
        return {
            "x": _w((lambda *s: torch.randn(*s, dtype=torch.bfloat16) * 0.05)(B, S, D)),
            "wqkv": _w((lambda *s: torch.randn(*s, dtype=torch.bfloat16) * 0.2)(D, 3 * D)),
            "w_phi": _w((lambda *s: torch.randn(*s, dtype=torch.bfloat16) * 0.2)(H, Dh, Dh) * (Dh ** -0.5)),
            "wo": _w((lambda *s: torch.randn(*s, dtype=torch.bfloat16) * 0.05)(D, D)),
        }
    _A_in = _A_setup()

    def _B_setup():
        B, S, D, F = 1, 2048, 448, 1792
        return {
            "x": _w((lambda *s: torch.randn(*s, dtype=torch.bfloat16) * 0.05)(B, S, D)),
            "w1": _w((lambda *s: torch.randn(*s, dtype=torch.bfloat16) * 0.05)(D, F)),
            "b1": _w((lambda *s: torch.randn(*s, dtype=torch.bfloat16) * 0.2)(F)),
            "w2": _w((lambda *s: torch.randn(*s, dtype=torch.bfloat16) * 0.05)(F, D)),
            "b2": _w((lambda *s: torch.randn(*s, dtype=torch.bfloat16) * 0.8)(D)),
        }
    _B_in = _B_setup()

    return {
        "A_x": _A_in["x"],
        "A_wqkv": _A_in["wqkv"],
        "A_w_phi": _A_in["w_phi"],
        "A_wo": _A_in["wo"],
        "B_x": _B_in["x"],
        "B_w1": _B_in["w1"],
        "B_b1": _B_in["b1"],
        "B_w2": _B_in["w2"],
        "B_b2": _B_in["b2"],
    }

def reference(A_x, A_wqkv, A_w_phi, A_wo, B_x, B_w1, B_b1, B_w2, B_b2):
    def _A_ref(x, wqkv, w_phi, wo):
        B, S, D = x.shape
        H = 16; Dh = D // H
        qkv = (lambda x, *d: x.permute(*d))((x @ wqkv).reshape(B, S, 3, H, Dh), 2, 0, 3, 1, 4)
        q, k, v = qkv[0], qkv[1], qkv[2]
        pq = torch.einsum("bhsd,hdf->bhsf", q, w_phi)
        pk = torch.einsum("bhsd,hdf->bhsf", k, w_phi)
        phi_q = torch.concatenate([_softmax(pq, axis=-1), _softmax(-pq, axis=-1)], axis=-1)
        phi_k = torch.concatenate([_softmax(pk, axis=-1), _softmax(-pk, axis=-1)], axis=-1)
        kv = torch.einsum("bhsf,bhsd->bhfd", phi_k, v)
        z  = torch.einsum("bhsf->bhf", phi_k)
        num = torch.einsum("bhqf,bhfd->bhqd", phi_q, kv)
        den = torch.einsum("bhqf,bhf->bhq", phi_q, z)[..., None] + 1e-6
        o = num / den
        return (lambda x, *d: x.permute(*d))(o, 0, 2, 1, 3).reshape(B, S, D) @ wo

    def _B_ref(x, w1, b1, w2, b2):
        h = torch.einsum("bsd,df->bsf", x, w1) + b1
        zero = torch.zeros_like(h)
        r = torch.where(h > zero, h, zero)
        h = r * r
        out = torch.einsum("bsf,fd->bsd", h, w2) + b2
        return out

    x = A_x
    xn = _layernorm(x, axis=-1)
    s = _A_ref(xn, A_wqkv, A_w_phi, A_wo)
    g = _silu(_B_ref(xn, B_w1, B_b1, B_w2, B_b2))
    return x + s * g
```

```cpp
#include <hip/hip_runtime.h>
#include <hip/hip_bf16.h>
#include <stdint.h>
typedef unsigned short u16;
__device__ __forceinline__ float b2f(u16 v){ return __uint_as_float(((unsigned)v)<<16); }
__device__ __forceinline__ u16 f2b(float f){ __hip_bfloat16 h = __float2bfloat16(f); return *(u16*)&h; }
constexpr int S=2048, D=448, H=16, DH=28, F=1792;

__global__ void ln_k(const u16* x, u16* xn){
  int r=blockIdx.x; int t=threadIdx.x;
  float v[7]; float s=0;
  for(int i=0;i<7;i++){ v[i]=b2f(x[r*D+t+64*i]); s+=v[i]; }
  for(int m=32;m;m>>=1) s+=__shfl_xor(s,m);
  float mean=s/D; float q=0;
  for(int i=0;i<7;i++){ float d=v[i]-mean; q+=d*d; }
  for(int m=32;m;m>>=1) q+=__shfl_xor(q,m);
  float rs=1.0f/sqrtf(q/D+1e-5f);
  for(int i=0;i<7;i++) xn[r*D+t+64*i]=f2b((v[i]-mean)*rs);
}
__global__ void gemm_k(const u16* A,const u16* B,const u16* bias,u16* Cb,float* Cf,int N,int K,int mode){
  int n=blockIdx.x*64+threadIdx.x; int m=blockIdx.y;
  if(n>=N) return;
  float acc=0;
  for(int k=0;k<K;k++) acc+=b2f(A[m*K+k])*b2f(B[k*N+n]);
  if(mode==0) Cb[m*N+n]=f2b(acc);
  else if(mode==1){ float h=acc+b2f(bias[n]); h=h>0?h:0; Cb[m*N+n]=f2b(h*h); }
  else if(mode==2){ float h=acc+b2f(bias[n]); Cf[m*N+n]=h/(1.0f+__expf(-h)); }
  else Cf[m*N+n]=acc;
}
__global__ void phi_k(const u16* qkv,const u16* wphi,float* phi,int which){
  int s=blockIdx.x; int h=threadIdx.x; if(h>=H) return;
  float p[DH];
  for(int f=0;f<DH;f++){ float a=0; for(int d=0;d<DH;d++) a+=b2f(qkv[s*3*D+which*D+h*DH+d])*b2f(wphi[(h*DH+d)*DH+f]); p[f]=a; }
  float mx=-1e30f,mn=1e30f; for(int f=0;f<DH;f++){mx=fmaxf(mx,p[f]);mn=fminf(mn,p[f]);}
  float s1=0,s2=0; for(int f=0;f<DH;f++){s1+=__expf(p[f]-mx); s2+=__expf(mn-p[f]);}
  for(int f=0;f<DH;f++){ phi[(s*H+h)*56+f]=__expf(p[f]-mx)/s1; phi[(s*H+h)*56+28+f]=__expf(mn-p[f])/s2; }
}
__global__ void kv_k(const float* phik,const u16* qkv,float* kv){
  int hf=blockIdx.x; int h=hf/56,f=hf%56; int d=threadIdx.x; if(d>28) return;
  float a=0;
  for(int s=0;s<S;s++){ float v= d<28? b2f(qkv[s*3*D+2*D+h*DH+d]) : 1.0f; a+=phik[(s*H+h)*56+f]*v; }
  kv[hf*32+d]=a;
}
__global__ void o_k(const float* phiq,const float* kv,u16* o){
  int s=blockIdx.x; int t=threadIdx.x; if(t>=D) return; int h=t/DH,d=t%DH;
  float n=0,dn=0; for(int f=0;f<56;f++){ float p=phiq[(s*H+h)*56+f]; n+=p*kv[(h*56+f)*32+d]; dn+=p*kv[(h*56+f)*32+28]; }
  o[s*D+t]=f2b(n/(dn+1e-6f));
}
__global__ void fin_k(const u16* x,const float* sv,const float* g,u16* out){
  int i=blockIdx.x*256+threadIdx.x; if(i<S*D) out[i]=f2b(b2f(x[i])+sv[i]*g[i]);
}
extern "C" void kernel_launch(void* const* d_in, const int* in_sizes, int n_in,
                              void* d_out, int out_size, void* d_ws, size_t ws_size,
                              hipStream_t stream) {
  const u16* x=(const u16*)d_in[0]; const u16* wqkv=(const u16*)d_in[1]; const u16* wphi=(const u16*)d_in[2]; const u16* wo=(const u16*)d_in[3];
  const u16* w1=(const u16*)d_in[5]; const u16* b1=(const u16*)d_in[6]; const u16* w2=(const u16*)d_in[7]; const u16* b2=(const u16*)d_in[8];
  char* ws=(char*)d_ws;
  u16* xn=(u16*)ws; u16* qkv=(u16*)(ws+(2<<20)); u16* hb=(u16*)(ws+(10<<20)); float* g=(float*)(ws+(20<<20)); float* sv=(float*)(ws+(24<<20));
  float* phiq=(float*)(ws+(28<<20)); float* phik=(float*)(ws+(36<<20)); float* kv=(float*)(ws+(44<<20)); u16* o=(u16*)(ws+(45<<20));
  ln_k<<<S,64,0,stream>>>(x,xn);
  gemm_k<<<dim3(21,S),64,0,stream>>>(xn,wqkv,nullptr,qkv,nullptr,3*D,D,0);
  gemm_k<<<dim3(28,S),64,0,stream>>>(xn,w1,b1,hb,nullptr,F,D,1);
  gemm_k<<<dim3(7,S),64,0,stream>>>(hb,w2,b2,nullptr,g,D,F,2);
  phi_k<<<S,64,0,stream>>>(qkv,wphi,phiq,0);
  phi_k<<<S,64,0,stream>>>(qkv,wphi,phik,1);
  kv_k<<<H*56,32,0,stream>>>(phik,qkv,kv);
  o_k<<<S,448,0,stream>>>(phiq,kv,o);
  gemm_k<<<dim3(7,S),64,0,stream>>>(o,wo,nullptr,nullptr,sv,D,D,3);
  fin_k<<<(S*D+255)/256,256,0,stream>>>(x,sv,g,(u16*)d_out);
}
```

```cpp
#include <hip/hip_runtime.h>
#include <hip/hip_bf16.h>
#include <stdint.h>

typedef unsigned short u16;
typedef uint32_t u32;
typedef uint64_t u64;
typedef __bf16 bf2 __attribute__((ext_vector_type(2)));
typedef __bf16 bf4 __attribute__((ext_vector_type(4)));
typedef __bf16 bf8 __attribute__((ext_vector_type(8)));
typedef float f4v __attribute__((ext_vector_type(4)));
typedef float f16v __attribute__((ext_vector_type(16)));
typedef u32 u2v __attribute__((ext_vector_type(2)));
typedef u32 u4v __attribute__((ext_vector_type(4)));
#define LAS __attribute__((address_space(3)))
#define GAS __attribute__((address_space(1)))
#define CAS __attribute__((address_space(4)))
#define DI __device__ __forceinline__

constexpr int S_ = 2048, D_ = 448, NH = 16, DH = 28, FF = 1792;
constexpr int XROW = D_ * 2;
constexpr int QROW = 3 * D_ * 2;
constexpr int W1ROW = FF * 2;

constexpr u32 WS_EPOCH = 0, WS_ERR = 128;
constexpr u32 FL0 = 4096, FLS = 128;
constexpr u32 FL_P0 = FL0;
constexpr u32 FL_C1 = FL_P0 + 128 * FLS;
constexpr u32 FL_H = FL_C1 + 1024 * FLS;
constexpr u32 FL_KV = FL_H + 1024 * FLS;
constexpr u32 FL_KR = FL_KV + 512 * FLS;
constexpr u32 FL_O = FL_KR + 128 * FLS;
constexpr u32 OFF_ST = FL_O + 1024 * FLS;
constexpr u32 OFF_WP = OFF_ST + S_ * 16;
constexpr u32 WP_HEAD = 84 * 1024, WP_Q = 56 * 1024;
constexpr u32 OFF_CSP = OFF_WP + NH * WP_HEAD;
constexpr u32 OFF_C1P = OFF_CSP + NH * 3 * 1024;
constexpr u32 OFF_H = OFF_C1P + 256 * 512;
constexpr u32 OFF_KVP = OFF_H + 64 * 112 * 1024;
constexpr u32 OFF_KVT = OFF_KVP + NH * 2 * 16 * 4096;
constexpr u32 OFF_O = OFF_KVT + NH * 4 * 1024;
constexpr u32 OFF_DIAG = OFF_O + 64 * 32 * 1024;
constexpr u32 WS_USED = OFF_DIAG + 256 * 8 * 64 * 8;

constexpr u32 SLOT = 16384;
constexpr u32 L_PHI = 8 * SLOT;
constexpr u32 L_V = L_PHI + 16384;
constexpr u32 L_AUG = L_V + 8192;
constexpr u32 LDS_BYTES = 163840;

constexpr float LOG2E = 1.4426950408889634f;
constexpr u32 SPIN_CAP = 1u << 18;

struct Params {
  const u16 *x, *wqkv, *wphi, *wo, *w1, *b1, *w2, *b2;
  u16* out;
  char* ws;
};

#define STAMP(id)

DI float bf_lo(u32 u) { return __uint_as_float(u << 16); }
DI float bf_hi(u32 u) { return __uint_as_float(u & 0xffff0000u); }
DI u32 pk2(float a, float b) {
  bf2 v = {(__bf16)a, (__bf16)b};
  return __builtin_bit_cast(u32, v);
}
DI u32 bf_rn(float a) { return pk2(a, 0.f) & 0xffffu; }
DI bf8 as_bf8(u4v v) { return __builtin_bit_cast(bf8, v); }
DI bf8 frag8(const f16v& a, int s) {
  u4v r = {pk2(a[8 * s], a[8 * s + 1]), pk2(a[8 * s + 2], a[8 * s + 3]), pk2(a[8 * s + 4], a[8 * s + 5]), pk2(a[8 * s + 6], a[8 * s + 7])};
  return as_bf8(r);
}
DI f16v mfma32(bf8 a, bf8 b, f16v c) { return __builtin_amdgcn_mfma_f32_32x32x16_bf16(a, b, c, 0, 0, 0); }
DI f4v mfma16(bf8 a, bf8 b, f4v c) { return __builtin_amdgcn_mfma_f32_16x16x32_bf16(a, b, c, 0, 0, 0); }
DI bf8 lds_frag(LAS const char* p) { return *(LAS const bf8*)p; }
DI bf8 lds_tr2(LAS const char* p0, LAS const char* p1) {
  bf4 a = __builtin_amdgcn_ds_read_tr16_b64_v4bf16((LAS bf4*)p0);
  bf4 b = __builtin_amdgcn_ds_read_tr16_b64_v4bf16((LAS bf4*)p1);
  return __builtin_shufflevector(a, b, 0, 1, 2, 3, 4, 5, 6, 7);
}
DI float xhalf_max(float v) {
  auto r = __builtin_amdgcn_permlane32_swap(__float_as_uint(v), __float_as_uint(v), false, false);
  return fmaxf(__uint_as_float(r[0]), __uint_as_float(r[1]));
}
DI float xhalf_min(float v) {
  auto r = __builtin_amdgcn_permlane32_swap(__float_as_uint(v), __float_as_uint(v), false, false);
  return fminf(__uint_as_float(r[0]), __uint_as_float(r[1]));
}
DI float xhalf_sum(float v) {
  auto r = __builtin_amdgcn_permlane32_swap(__float_as_uint(v), __float_as_uint(v), false, false);
  return __uint_as_float(r[0]) + __uint_as_float(r[1]);
}
DI float xhalf_hi(float v) {
  auto r = __builtin_amdgcn_permlane32_swap(__float_as_uint(v), __float_as_uint(v), false, false);
  return __uint_as_float(r[1]);
}
DI void bar() { asm volatile("s_waitcnt lgkmcnt(0)\n\ts_barrier" ::: "memory"); }
DI void bar_only() { asm volatile("s_barrier" ::: "memory"); }
template <int N> DI void vmwait() { asm volatile("s_waitcnt vmcnt(%0)" ::"i"(N) : "memory"); }
#define SCHED() __builtin_amdgcn_sched_barrier(0)

DI void dma(u32 voff, const void* sbase, u32 ldsaddr) {
  asm volatile("s_mov_b32 m0, %2\n\ts_nop 0\n\tglobal_load_lds_dwordx4 %0, %1" ::"v"(voff), "s"(sbase), "s"(ldsaddr) : "memory");
}
DI void dma_sc1(u32 voff, const void* sbase, u32 ldsaddr) {
  asm volatile("s_mov_b32 m0, %2\n\ts_nop 0\n\tglobal_load_lds_dwordx4 %0, %1 sc1" ::"v"(voff), "s"(sbase), "s"(ldsaddr) : "memory");
}

typedef __amdgpu_buffer_rsrc_t rsrc_t;
DI u4v ld128(rsrc_t r, u32 off) { return __builtin_amdgcn_raw_buffer_load_b128(r, off, 0, 16); }
DI u32 ld32(rsrc_t r, u32 off) { return __builtin_amdgcn_raw_buffer_load_b32(r, off, 0, 16); }
DI void st128(rsrc_t r, u32 off, u4v v) { __builtin_amdgcn_raw_buffer_store_b128(v, r, off, 0, 16); }
DI void st64(rsrc_t r, u32 off, u2v v) { __builtin_amdgcn_raw_buffer_store_b64(v, r, off, 0, 16); }
DI void st32(rsrc_t r, u32 off, u32 v) { __builtin_amdgcn_raw_buffer_store_b32(v, r, off, 0, 16); }

DI u64 tag64(u32 t) { return (u64)t | ((u64)(t * 0x9E3779B1u + 0x7F4A7C15u) << 32); }

DI void flag_set(char* ws, u32 off, u64 tag, int lane) {
  asm volatile("s_waitcnt vmcnt(0)" ::: "memory");
  if (lane == 0) __hip_atomic_store((GAS u64*)(ws + off), tag, __ATOMIC_RELAXED, __HIP_MEMORY_SCOPE_AGENT);
}
DI void flag_wait(char* ws, u32 off, int n, u64 tag, int lane, u32 code) {
  GAS u64* ptr = (GAS u64*)(ws + off);
#pragma unroll 1
  for (u32 spin = 0;; ++spin) {
    u64 v = tag;
    if (lane < n) v = __hip_atomic_load(ptr, __ATOMIC_RELAXED, __HIP_MEMORY_SCOPE_AGENT);
    if (__all(v == tag)) break;
    if (spin > SPIN_CAP) {
      if (lane == 0) __hip_atomic_store((GAS u32*)(ws + WS_ERR), code, __ATOMIC_RELAXED, __HIP_MEMORY_SCOPE_AGENT);
      break;
    }
    __builtin_amdgcn_s_sleep(1);
  }
  asm volatile("" ::: "memory");
}
DI void stat_wait(char* ws, rsrc_t rs, int token, u64 tag, int lane, float& mean, float& sigma) {
#pragma unroll 1
  for (u32 spin = 0;; ++spin) {
    u4v g = ld128(rs, OFF_ST + token * 16);
    mean = __uint_as_float(g[0]);
    sigma = __uint_as_float(g[1]);
    bool ok = g[2] == (u32)tag && g[3] == (u32)(tag >> 32);
    if (__all(ok)) break;
    if (spin > SPIN_CAP) {
      if (lane == 0) __hip_atomic_store((GAS u32*)(ws + WS_ERR), 99u, __ATOMIC_RELAXED, __HIP_MEMORY_SCOPE_AGENT);
      break;
    }
    __builtin_amdgcn_s_sleep(1);
  }
}
DI void split2(float v, u32& hi, u32& lo) {
  hi = bf_rn(v);
  lo = bf_rn(v - __uint_as_float(hi << 16));
}
DI bf8 aug_w(float cs, u32 beta, int hh) {
  u32 hi, lo;
  split2(cs, hi, lo);
  u4v r = {hi | (hi << 16), lo | (beta << 16), beta, 0u};
  if (hh) r = u4v{0u, 0u, 0u, 0u};
  return as_bf8(r);
}

DI void issue_x(const Params& p, int i, int gc, int lw, u32 vxe, u32 vxo, u32 slotaddr) {
  const char* b = (const char*)p.x + (size_t)(128 * i + 32 * lw) * XROW + gc * 128;
#pragma unroll
  for (int q = 0; q < 4; ++q) dma((q & 1) ? vxo : vxe, b + q * 8 * XROW, slotaddr + (4 * lw + q) * 1024);
}
DI void issue_w1(const Params& p, int j, int gc, int lw, u32 vw, u32 slotaddr) {
  const char* b = (const char*)p.w1 + (size_t)(64 * gc + 16 * lw) * W1ROW + j * 256;
#pragma unroll
  for (int q = 0; q < 4; ++q) dma(vw, b + q * 4 * W1ROW, slotaddr + (4 * lw + q) * 1024);
}
template <int N> DI void issue_lin(const char* src, u32 vl, u32 ldsaddr) {
#pragma unroll
  for (int q = 0; q < N; ++q) dma_sc1(vl, src + q * 1024, ldsaddr + q * 1024);
}
DI void issue_wb(const Params& p, int g, int cc, int lw, u32 vb, u32 vb2, u32 slotaddr) {
  if (g < 14) {
    const char* b = (const char*)p.w2 + (size_t)(128 * g + 32 * lw) * XROW + cc * 128;
#pragma unroll
    for (int q = 0; q < 4; ++q) dma(vb, b + q * 8 * XROW, slotaddr + (4 * lw + q) * 1024);
  } else {
    const char* b = (const char*)p.wo + (size_t)(28 * (4 * (g - 14) + lw)) * XROW + cc * 128;
#pragma unroll
    for (int q = 0; q < 4; ++q) dma(q == 3 ? vb2 : vb, b + q * 8 * XROW, slotaddr + (4 * lw + q) * 1024);
  }
}
DI void issue_hb(const Params& p, int g, int m64, int lw, u32 vl, u32 slotaddr) {
  const int m = lw >> 1, k0 = (lw & 1) * 4;
  const char* b = (g < 14) ? p.ws + OFF_H + (size_t)((2 * m64 + m) * 112 + 8 * g + k0) * 1024
                           : p.ws + OFF_O + (size_t)((2 * m64 + m) * 32 + 8 * (g - 14) + k0) * 1024;
  issue_lin<4>(b, vl, slotaddr + lw * 4096);
}
DI u32 wslot(int g) { int s = g % 5; return (u32)((s + 6) & 7) * SLOT; }
DI u32 hslot(int g) { return (u32)(3 + g % 3) * SLOT; }

DI void loader_main(const Params& p, u32 lb, int i, int j, bool helper, int lw, int lane, int wave) {
  const int g0 = i % 7;
  const int m64 = 2 * i + (j >= 7 ? 1 : 0), cc = j >= 7 ? j - 7 : j;
  const u32 vxe = (lane >> 3) * XROW + (((lane & 7) ^ (lane >> 4)) << 4);
  const u32 vxo = (lane >> 3) * XROW + (((lane & 7) ^ (lane >> 4) ^ 4) << 4);
  const u32 vw = (lane >> 4) * W1ROW + (((lane & 15) ^ ((lane >> 4) << 2)) << 4);
  const u32 vl = lane * 16;
  const u32 bchunk = ((lane & 7) ^ (((lane >> 4) & 1) << 2)) << 4;
  const u32 vb = (lane >> 3) * XROW + bchunk;
  const u32 vb2 = min(lane >> 3, 3) * XROW + bchunk;
  auto gch = [&](int c) { int t = c + g0; return t >= 7 ? t - 7 : t; };

  if (helper) {
    bar_only();
#pragma unroll 1
    for (int c = 0; c < 7; ++c) issue_x(p, i, gch(c), lw, vxe, vxo, lb + c * SLOT);
  } else {
#pragma unroll 1
    for (int c = 0; c < 4; ++c) {
      issue_x(p, i, gch(c), lw, vxe, vxo, lb + 2 * c * SLOT);
      issue_w1(p, j, gch(c), lw, vw, lb + (2 * c + 1) * SLOT);
    }
  }
  const u32 E = *(CAS const u32*)(p.ws + WS_EPOCH);
  const u64 tag = tag64(E + 1);
  STAMP(1);
  const char* wp = p.ws + OFF_WP + (size_t)j * WP_HEAD;
  if (!helper) {
    vmwait<24>(); bar_only();
    STAMP(2);
    vmwait<16>(); bar_only();
#pragma unroll 1
    for (int c = 4; c < 7; ++c) {
      issue_x(p, i, gch(c), lw, vxe, vxo, lb + ((2 * c) & 7) * SLOT);
      issue_w1(p, j, gch(c), lw, vw, lb + ((2 * c + 1) & 7) * SLOT);
      vmwait<16>(); bar_only();
    }
    issue_wb(p, 0, cc, lw, vb, vb2, lb + wslot(0));
    issue_wb(p, 1, cc, lw, vb, vb2, lb + wslot(1));
    vmwait<16>(); bar_only();
  } else {
    vmwait<24>(); bar_only();
    vmwait<20>(); bar_only();
    vmwait<16>(); bar_only();
    vmwait<12>(); bar_only();
    vmwait<8>(); bar_only();
    vmwait<4>(); bar_only();
  }
  STAMP(3);
  flag_wait(p.ws, FL_P0 + (j * 8 + lane) * FLS, 8, tag, lane, 1);
  STAMP(4);
  issue_lin<2>(wp + gch(0) * 8192 + lw * 2048, vl, lb + 0 * SLOT + lw * 2048);
  issue_lin<2>(wp + gch(1) * 8192 + lw * 2048, vl, lb + 0 * SLOT + 8192 + lw * 2048);
  issue_lin<2>(wp + gch(2) * 8192 + lw * 2048, vl, lb + 1 * SLOT + lw * 2048);
  issue_lin<2>(wp + gch(3) * 8192 + lw * 2048, vl, lb + 1 * SLOT + 8192 + lw * 2048);
  vmwait<8>(); bar_only();
  issue_lin<2>(wp + gch(4) * 8192 + lw * 2048, vl, lb + 2 * SLOT + lw * 2048);
  issue_lin<2>(wp + gch(5) * 8192 + lw * 2048, vl, lb + 2 * SLOT + 8192 + lw * 2048);
  issue_lin<2>(wp + gch(6) * 8192 + lw * 2048, vl, lb + 3 * SLOT + lw * 2048);
  vmwait<0>(); bar_only();
  STAMP(5);
#pragma unroll
  for (int c = 0; c < 7; ++c) issue_lin<1>(wp + WP_Q + gch(c) * 4096 + lw * 1024, vl, lb + 4 * SLOT + c * 4096 + lw * 1024);
  bar_only();
  STAMP(6);
  if (!helper) {
    issue_wb(p, 2, cc, lw, vb, vb2, lb + wslot(2));
    issue_wb(p, 3, cc, lw, vb, vb2, lb + wslot(3));
    issue_wb(p, 4, cc, lw, vb, vb2, lb + wslot(4));
    flag_wait(p.ws, FL_H + ((i * 16 + (lane >> 1)) * 4 + 2 * (m64 & 1) + (lane & 1)) * FLS, 28, tag, lane, 2);
    STAMP(7);
    issue_hb(p, 0, m64, lw, vl, lb + hslot(0));
    vmwait<4>();
  } else {
    vmwait<0>();
  }
  bar_only();
  bar_only();
  if (helper) return;
  issue_hb(p, 1, m64, lw, vl, lb + hslot(1));
  issue_hb(p, 2, m64, lw, vl, lb + hslot(2));
  {
    const char* b = (const char*)p.x + (size_t)(64 * m64 + 16 * lw) * XROW + cc * 128;
    dma(vxe, b, lb + L_V + lw * 2048);
    dma(vxo, b + 8 * XROW, lb + L_V + lw * 2048 + 1024);
  }
  STAMP(8);
  vmwait<10>(); bar_only();
  vmwait<4>(); bar_only();
#pragma unroll 1
  for (int g = 1; g < 17; ++g) {
    if (g == 12) {
      flag_wait(p.ws, FL_O + ((i * 16 + (lane >> 1)) * 4 + 2 * (m64 & 1) + (lane & 1)) * FLS, 32, tag, lane, 3);
      STAMP(9);
    }
    if (g + 2 < 18) issue_hb(p, g + 2, m64, lw, vl, lb + hslot(g + 2));
    if (g + 4 < 18) issue_wb(p, g + 4, cc, lw, vb, vb2, lb + wslot(g + 4));
    if (g == 1) vmwait<8>();
    else if (g < 14) vmwait<12>();
    else if (g == 14) vmwait<8>();
    else if (g == 15) vmwait<4>();
    else vmwait<0>();
    bar_only();
  }
  STAMP(10);
  bar_only();
  {
    LAS const char* lds = (LAS const char*)(size_t)lb;
    const int row = 8 * wave + (lane >> 3);
    u4v v = *(LAS const u4v*)(lds + L_V + row * 128 + (((lane & 7) ^ ((row >> 1) & 7)) << 4));
    *(GAS u4v*)((char*)p.out + (size_t)(64 * m64 + row) * XROW + cc * 128 + (lane & 7) * 16) = v;
  }
  if (blockIdx.x == 0 && lw == 0 && lane == 0)
    __hip_atomic_store((GAS u32*)(p.ws + WS_EPOCH), E + 1, __ATOMIC_RELAXED, __HIP_MEMORY_SCOPE_AGENT);
  STAMP(11);
}

DI void exp_pair(const f16v& a, float c, f16v& ep, f16v& en) {
  float mx = fmaxf(a[0], a[1]), mn = fminf(a[0], a[1]);
#pragma unroll
  for (int r = 2; r < 16; r += 2) {
    mx = fmaxf(fmaxf(mx, a[r]), a[r + 1]);
    mn = fminf(fminf(mn, a[r]), a[r + 1]);
  }
  mx = xhalf_max(mx);
  mn = xhalf_min(mn);
  const float bp = -mx * c, bn = mn * c;
#pragma unroll
  for (int r = 0; r < 16; ++r) {
    ep[r] = __builtin_amdgcn_exp2f(__builtin_fmaf(a[r], c, bp));
    en[r] = __builtin_amdgcn_exp2f(__builtin_fmaf(a[r], -c, bn));
  }
}
DI float sum28(const f16v& e, int hh) {
  float s0 = (e[0] + e[1]) + (e[2] + e[3]), s1 = (e[4] + e[5]) + (e[6] + e[7]), s2 = (e[8] + e[9]) + (e[10] + e[11]);
  float s3 = (e[12] + e[13]) + (e[14] + e[15]);
  return xhalf_sum((s0 + s1) + (s2 + (hh ? 0.f : s3)));
}

DI void fold_load(const Params& p, int rho, int w, int lane, u4v (&a)[11], u32 (&bw)[2][4]) {
  const int pr = rho >> 2, u = rho & 3, head = 2 * pr + (w & 1), pk = w >> 1;
  const int r16 = lane & 15, dg = lane >> 4;
  const char* src = (const char*)p.wqkv + (size_t)(112 * u + r16) * QROW + head * 56 + (dg == 3 ? 40 : dg * 16);
#pragma unroll
  for (int t = 0; t < 7; ++t) a[t] = *(GAS const u4v*)(src + (size_t)t * 16 * QROW + pk * XROW);
  const int v0 = pk ? 4 : 0;
#pragma unroll
  for (int t = 0; t < 4; ++t) {
    const int kk = min(v0 + t, 6);
    a[7 + t] = *(GAS const u4v*)(src + (size_t)kk * 16 * QROW + 2 * XROW);
  }
#pragma unroll
  for (int ft = 0; ft < 2; ++ft) {
    const int f = min(16 * ft + r16, 27);
#pragma unroll
    for (int q = 0; q < 4; ++q) {
      const int d0 = 8 * dg + 2 * q;
      const GAS u16* wp = (const GAS u16*)p.wphi + (head * 28 + min(d0, 26)) * 28 + f;
      u32 lo = wp[0], hi = wp[28];
      bw[ft][q] = d0 < 28 ? (lo | (hi << 16)) : 0u;
    }
  }
}
DI void fold_run(const Params& p, rsrc_t rs, int rho, int w, int lane, u64 tag, const u4v (&a)[11], const u32 (&bw)[2][4]) {
  const int pr = rho >> 2, u = rho & 3, head = 2 * pr + (w & 1), pk = w >> 1;
  const int r16 = lane & 15, dg = lane >> 4;
  const int v0 = pk ? 4 : 0;
  u32 bi[2][4];
#pragma unroll
  for (int ft = 0; ft < 2; ++ft) {
    const int f = 16 * ft + r16;
#pragma unroll
    for (int q = 0; q < 4; ++q) {
      const int d0 = 8 * dg + 2 * q;
      bi[ft][q] = f < 28 ? ((d0 == f ? 0x3f80u : 0u) | (d0 + 1 == f ? 0x3f800000u : 0u)) : 0u;
    }
  }
  u4v o[11];
  const u32 wbase = OFF_WP + head * WP_HEAD;
#pragma unroll
  for (int t = 0; t < 11; ++t) {
    u4v av = a[t];
    if (dg == 3) av = u4v{av[2], av[3], 0u, 0u};
    const bool isv = t >= 7;
    u4v b0 = isv ? u4v{bi[0][0], bi[0][1], bi[0][2], bi[0][3]} : u4v{bw[0][0], bw[0][1], bw[0][2], bw[0][3]};
    u4v b1 = isv ? u4v{bi[1][0], bi[1][1], bi[1][2], bi[1][3]} : u4v{bw[1][0], bw[1][1], bw[1][2], bw[1][3]};
    const f4v z = {0.f, 0.f, 0.f, 0.f};
    f4v d0 = mfma16(as_bf8(av), as_bf8(b0), z), d1 = mfma16(as_bf8(av), as_bf8(b1), z);
    auto s0 = __builtin_amdgcn_permlane16_swap(pk2(d0[0], d0[1]), pk2(d1[0], d1[1]), false, false);
    auto s1 = __builtin_amdgcn_permlane16_swap(pk2(d0[2], d0[3]), pk2(d1[2], d1[3]), false, false);
    o[t] = u4v{s0[0], s1[0], s0[1], s1[1]};
    const int kk = isv ? v0 + (t - 7) : t;
    const int ks = 7 * u + kk;
    const u32 off = isv ? wbase + ks * 2048 + 1024 : (pk ? wbase + ks * 2048 : wbase + WP_Q + ks * 1024);
    if (!isv || kk < 7) st128(rs, off + lane * 16, o[t]);
  }
  float cs0 = 0.f, cs1 = 0.f;
#pragma unroll
  for (int t = 0; t < 11; ++t) {
    float s = ((bf_lo(o[t][0]) + bf_hi(o[t][0])) + (bf_lo(o[t][1]) + bf_hi(o[t][1]))) +
              ((bf_lo(o[t][2]) + bf_hi(o[t][2])) + (bf_lo(o[t][3]) + bf_hi(o[t][3])));
    if (t < 7) cs0 += s;
    else cs1 += (t == 10 && pk) ? 0.f : s;
  }
  cs0 = xhalf_sum(cs0);
  cs1 = xhalf_sum(cs1);
  if (lane < 32) {
    st32(rs, OFF_CSP + ((head * 3 + (pk ? 0 : 2)) * 8 + u) * 128 + lane * 4, __float_as_uint(cs0));
    st32(rs, OFF_CSP + ((head * 3 + 1) * 8 + 2 * u + pk) * 128 + lane * 4, __float_as_uint(cs1));
  }
  flag_set(p.ws, FL_P0 + (head * 8 + u * 2 + pk) * FLS, tag, lane);
}

template <int C>
DI void tick_f(LAS const char* lds, const u32 (&xa)[4], const u32 (&wa)[4], u32 xs, u32 xs_next, bf8 (&x)[7][4], bf8 (&wf)[2][4], f16v (&acc)[4]) {
  constexpr u32 ws = ((2 * C + 1) & 7) * SLOT, wsn = ((2 * C + 3) & 7) * SLOT;
#pragma unroll
  for (int s = 0; s < 4; ++s) {
    if (s < 3) {
      x[C][s + 1] = lds_frag(lds + xs + xa[s + 1]);
#pragma unroll
      for (int n = 0; n < 4; ++n) wf[(s + 1) & 1][n] = lds_tr2(lds + ws + wa[n] + 4096 * (s + 1), lds + ws + wa[n] + 4096 * (s + 1) + 1024);
    } else if (C < 6) {
      bar();
      x[C < 6 ? C + 1 : C][0] = lds_frag(lds + xs_next + xa[0]);
#pragma unroll
      for (int n = 0; n < 4; ++n) wf[0][n] = lds_tr2(lds + wsn + wa[n], lds + wsn + wa[n] + 1024);
    }
#pragma unroll
    for (int n = 0; n < 4; ++n) acc[n] = mfma32(wf[s & 1][n], x[C][s], acc[n]);
#pragma unroll
    for (int n = 0; n < 4; ++n) {
      __builtin_amdgcn_sched_group_barrier(0x8, 1, 0);
      __builtin_amdgcn_sched_group_barrier(0x100, 3, 0);
    }
    SCHED();
  }
}

DI void compute_main(const Params& p, LAS char* lds, int i, int j, bool helper, int w, int lane, int wave) {
  const rsrc_t rs = __builtin_amdgcn_make_buffer_rsrc(p.ws, 0, WS_USED, 0x00020000);
  const int hh = lane >> 5, l31 = lane & 31;
  const u32 E = *(CAS const u32*)(p.ws + WS_EPOCH);
  u32 xs7[7];
  const int stok = 128 * i + 8 * j + 2 * w + hh;
#pragma unroll
  for (int m = 0; m < 7; ++m) xs7[m] = *(GAS const u32*)((const char*)p.x + (size_t)stok * XROW + m * 128 + l31 * 4);
  const u64 tag = tag64(E + 1);
  u4v fa[11];
  u32 fbw[2][4];
  if (helper) {
    fold_load(p, 2 * i + (j - 14), w, lane, fa, fbw);
    bar_only();
  }
  {
    float s = 0.f;
#pragma unroll
    for (int m = 0; m < 7; ++m) s += bf_lo(xs7[m]) + bf_hi(xs7[m]);
#pragma unroll
    for (int d = 1; d < 32; d <<= 1) s += __shfl_xor(s, d);
    const float mean = s * (1.f / D_);
    float q = 0.f;
#pragma unroll
    for (int m = 0; m < 7; ++m) {
      float a = bf_lo(xs7[m]) - mean, b = bf_hi(xs7[m]) - mean;
      q += a * a + b * b;
    }
#pragma unroll
    for (int d = 1; d < 32; d <<= 1) q += __shfl_xor(q, d);
    const float sigma = __builtin_sqrtf(q * (1.f / D_) + 1e-5f);
    if (l31 == 0) st128(rs, OFF_ST + stok * 16, u4v{__float_as_uint(mean), __float_as_uint(sigma), (u32)tag, (u32)(tag >> 32)});
  }
  if (helper) fold_run(p, rs, 2 * i + (j - 14), w, lane, tag, fa, fbw);
  STAMP(1);
  u32 xa[4], wa[4];
  {
    const int row = 32 * w + l31;
    const u32 b = row * 128 + ((hh ^ ((row >> 1) & 7)) << 4);
#pragma unroll
    for (int s = 0; s < 4; ++s) xa[s] = b ^ (32 * s);
    const int q4 = (lane & 15) >> 2, pp = lane & 3, blk = (lane >> 4) & 1;
#pragma unroll
    for (int n = 0; n < 4; ++n) {
      const int nt = (n + w) & 3;
      wa[n] = (8 * hh + q4) * 256 + (4 * (nt ^ q4) + 2 * blk + (pp >> 1)) * 16 + 8 * (pp & 1);
    }
  }
  auto xslot = [&](int c) -> u32 { return helper ? c * SLOT : ((2 * c) & 7) * SLOT; };
  bf8 x[7][4], wf[2][4];
  f16v acc[4];
#pragma unroll
  for (int n = 0; n < 4; ++n)
#pragma unroll
    for (int r = 0; r < 16; ++r) acc[n][r] = 0.f;

  bar();
  STAMP(2);
  x[0][0] = lds_frag(lds + xslot(0) + xa[0]);
#pragma unroll
  for (int n = 0; n < 4; ++n) wf[0][n] = lds_tr2(lds + SLOT + wa[n], lds + SLOT + wa[n] + 1024);
  {
    const int beta = i / 7;
    const u32 one0 = beta == 0 ? 0x3f803f80u : 0u, one1 = beta == 1 ? 0x3f803f80u : 0u;
    f16v cs;
#pragma unroll
    for (int r = 0; r < 16; ++r) cs[r] = 0.f;
    constexpr u32 ws = SLOT, wsn = 3 * SLOT;
    const u32 xs = xslot(0), xsn = xslot(1);
#pragma unroll
    for (int s = 0; s < 4; ++s) {
      if (s < 3) {
        x[0][s + 1] = lds_frag(lds + xs + xa[s + 1]);
#pragma unroll
        for (int n = 0; n < 4; ++n) wf[(s + 1) & 1][n] = lds_tr2(lds + ws + wa[n] + 4096 * (s + 1), lds + ws + wa[n] + 4096 * (s + 1) + 1024);
      } else {
        bar();
        x[1][0] = lds_frag(lds + xsn + xa[0]);
#pragma unroll
        for (int n = 0; n < 4; ++n) wf[0][n] = lds_tr2(lds + wsn + wa[n], lds + wsn + wa[n] + 1024);
      }
#pragma unroll
      for (int n = 0; n < 4; ++n) acc[n] = mfma32(wf[s & 1][n], x[0][s], acc[n]);
      const u32 o = s < 2 ? one0 : one1;
      cs = mfma32(as_bf8(u4v{o, o, o, o}), wf[s & 1][0], cs);
      SCHED();
    }
    if (!helper && i < 14 && lane < 32) st32(rs, OFF_C1P + (j * 16 + i) * 512 + w * 128 + lane * 4, __float_as_uint(cs[0]));
  }
  tick_f<1>(lds, xa, wa, xslot(1), xslot(2), x, wf, acc);
  if (!helper && i < 14) flag_set(p.ws, FL_C1 + ((j * 16 + i) * 4 + w) * FLS, tag, lane);
  tick_f<2>(lds, xa, wa, xslot(2), xslot(3), x, wf, acc);
  tick_f<3>(lds, xa, wa, xslot(3), xslot(4), x, wf, acc);
  tick_f<4>(lds, xa, wa, xslot(4), xslot(5), x, wf, acc);
  float mean, sigma;
  stat_wait(p.ws, rs, 128 * i + 32 * w + l31, tag, lane, mean, sigma);
  bf8 xaug;
  {
    u32 mh, ml, sh, sl;
    split2(-mean, mh, ml);
    split2(sigma, sh, sl);
    u4v r = {mh | (ml << 16), mh | (sh << 16), sl, 0u};
    if (hh) r = u4v{0u, 0u, 0u, 0u};
    xaug = as_bf8(r);
  }
  const float rstd = __builtin_amdgcn_rcpf(sigma);
  if (!helper) {
    flag_wait(p.ws, FL_C1 + ((j * 16 + lane) * 4 + w) * FLS, 14, tag, lane, 4);
    float c = 0.f;
#pragma unroll
    for (int q = 0; q < 7; ++q) c += __uint_as_float(ld32(rs, OFF_C1P + (j * 16 + 7 * hh + q) * 512 + w * 128 + l31 * 4));
    c = xhalf_sum(c);
    const u32 beta = *((const GAS u16*)p.b1 + j * 128 + w * 32 + l31);
    *(LAS bf8*)(lds + L_AUG + w * 1024 + lane * 16) = aug_w(c, beta, hh);
  }
  tick_f<5>(lds, xa, wa, xslot(5), xslot(6), x, wf, acc);
  STAMP(3);
  u32 cq[2], ck[2], cv[4];
#pragma unroll
  for (int q = 0; q < 2; ++q) {
    cq[q] = ld32(rs, OFF_CSP + ((j * 3 + 2) * 8 + 2 * hh + q) * 128 + l31 * 4);
    ck[q] = ld32(rs, OFF_CSP + ((j * 3 + 0) * 8 + 2 * hh + q) * 128 + l31 * 4);
  }
#pragma unroll
  for (int q = 0; q < 4; ++q) cv[q] = ld32(rs, OFF_CSP + ((j * 3 + 1) * 8 + 4 * hh + q) * 128 + l31 * 4);
  tick_f<6>(lds, xa, wa, 0 + xslot(6), 0, x, wf, acc);
  STAMP(4);
  if (!helper) {
#pragma unroll
    for (int n = 0; n < 4; ++n) acc[n] = mfma32(lds_frag(lds + L_AUG + ((n + w) & 3) * 1024 + lane * 16), xaug, acc[n]);
    const u32 hb = OFF_H + ((4 * i + w) * 112 + 8 * j) * 1024 + lane * 16;
#pragma unroll
    for (int n = 0; n < 4; ++n) {
#pragma unroll
      for (int r = 0; r < 16; ++r) {
        const float t = fmaxf(acc[n][r], 0.f);
        acc[n][r] = t * t;
      }
#pragma unroll
      for (int s = 0; s < 2; ++s) st128(rs, hb + (2 * ((n + w) & 3) + s) * 1024, __builtin_bit_cast(u4v, frag8(acc[n], s)));
    }
  }
  const bf8 augq = aug_w(xhalf_sum(__uint_as_float(cq[0]) + __uint_as_float(cq[1])), 0u, hh);
  const bf8 augk = aug_w(xhalf_sum(__uint_as_float(ck[0]) + __uint_as_float(ck[1])), 0u, hh);
  const bf8 augv = aug_w(xhalf_sum((__uint_as_float(cv[0]) + __uint_as_float(cv[1])) + (__uint_as_float(cv[2]) + __uint_as_float(cv[3]))),
                         l31 == 28 ? 0x3f80u : 0u, hh);
  STAMP(5);
  bar();
  STAMP(6);
  f16v ak, av;
#pragma unroll
  for (int r = 0; r < 16; ++r) { ak[r] = 0.f; av[r] = 0.f; }
  {
    LAS const char* wb = lds + lane * 16;
    bf8 fk[2], fv[2];
    fk[0] = lds_frag(wb);
    fv[0] = lds_frag(wb + 1024);
#pragma unroll
    for (int ks = 0; ks < 28; ++ks) {
      if (ks < 27) {
        fk[(ks + 1) & 1] = lds_frag(wb + (ks + 1) * 2048);
        fv[(ks + 1) & 1] = lds_frag(wb + (ks + 1) * 2048 + 1024);
      }
      ak = mfma32(fk[ks & 1], x[ks >> 2][ks & 3], ak);
      av = mfma32(fv[ks & 1], x[ks >> 2][ks & 3], av);
      __builtin_amdgcn_sched_group_barrier(0x8, 1, 0);
      __builtin_amdgcn_sched_group_barrier(0x100, 1, 0);
      __builtin_amdgcn_sched_group_barrier(0x8, 1, 0);
      __builtin_amdgcn_sched_group_barrier(0x100, 1, 0);
      SCHED();
    }
    ak = mfma32(augk, xaug, ak);
    av = mfma32(augv, xaug, av);
  }
  asm volatile("s_waitcnt vmcnt(0)" ::: "memory");
  bar();
  STAMP(7);
  if (!helper && lane == 0)
    __hip_atomic_store((GAS u64*)(p.ws + FL_H + ((i * 16 + j) * 4 + w) * FLS), tag, __ATOMIC_RELAXED, __HIP_MEMORY_SCOPE_AGENT);
  const float cexp = rstd * LOG2E;
  {
    f16v ep, en;
    exp_pair(ak, cexp, ep, en);
    const float ap = rstd * __builtin_amdgcn_rcpf(sum28(ep, hh)), an = rstd * __builtin_amdgcn_rcpf(sum28(en, hh));
    const int row = 32 * w + l31;
    LAS char* base = lds + row * 64 + 8 * hh;
    const int key = (l31 >> 1) & 3;
#pragma unroll
    for (int q = 0; q < 4; ++q) {
      const u32 o = (q ^ key) << 4;
      *(LAS u2v*)(base + L_PHI + o) = u2v{pk2(ep[4 * q] * ap, ep[4 * q + 1] * ap), pk2(ep[4 * q + 2] * ap, ep[4 * q + 3] * ap)};
      *(LAS u2v*)(base + L_PHI + 8192 + o) = u2v{pk2(en[4 * q] * an, en[4 * q + 1] * an), pk2(en[4 * q + 2] * an, en[4 * q + 3] * an)};
      *(LAS u2v*)(base + L_V + o) = u2v{pk2(av[4 * q], av[4 * q + 1]), pk2(av[4 * q + 2], av[4 * q + 3])};
    }
  }
  STAMP(8);
  bar();
  STAMP(9);
  if (w < 2) {
    const int q4 = (lane & 15) >> 2, pp = lane & 3, blk = (lane >> 4) & 1;
    u32 ta[2];
#pragma unroll
    for (int t = 0; t < 2; ++t) {
      const int row = 8 * hh + 4 * t + q4;
      ta[t] = row * 64 + (((2 * blk + (pp >> 1)) ^ ((row >> 1) & 3)) << 4) + 8 * (pp & 1);
    }
    f16v kv;
#pragma unroll
    for (int r = 0; r < 16; ++r) kv[r] = 0.f;
    LAS const char* pa = lds + L_PHI + w * 8192;
    LAS const char* pb = lds + L_V;
#pragma unroll
    for (int ks = 0; ks < 8; ++ks)
      kv = mfma32(lds_tr2(pa + ta[0] + ks * 1024, pa + ta[1] + ks * 1024), lds_tr2(pb + ta[0] + ks * 1024, pb + ta[1] + ks * 1024), kv);
    const u32 o = OFF_KVP + ((j * 2 + w) * 16 + i) * 4096 + lane * 16;
#pragma unroll
    for (int q = 0; q < 4; ++q)
      st128(rs, o + q * 1024, u4v{__float_as_uint(kv[4 * q]), __float_as_uint(kv[4 * q + 1]), __float_as_uint(kv[4 * q + 2]), __float_as_uint(kv[4 * q + 3])});
  }
  f16v aq;
#pragma unroll
  for (int r = 0; r < 16; ++r) aq[r] = 0.f;
  {
    LAS const char* wb = lds + 4 * SLOT + lane * 16;
    bf8 fq[2];
    fq[0] = lds_frag(wb);
#pragma unroll
    for (int ks = 0; ks < 28; ++ks) {
      if (ks < 27) fq[(ks + 1) & 1] = lds_frag(wb + (ks + 1) * 1024);
      aq = mfma32(fq[ks & 1], x[ks >> 2][ks & 3], aq);
      SCHED();
    }
    aq = mfma32(augq, xaug, aq);
  }
  if (w < 2) flag_set(p.ws, FL_KV + ((j * 2 + w) * 16 + i) * FLS, tag, lane);
  bar();
  STAMP(10);
  bf8 eq[2][2];
  {
    f16v ep, en;
    exp_pair(aq, cexp, ep, en);
    eq[0][0] = frag8(ep, 0); eq[0][1] = frag8(ep, 1);
    eq[1][0] = frag8(en, 0); eq[1][1] = frag8(en, 1);
  }
  STAMP(11);
  const int m64 = 2 * i + (j >= 7 ? 1 : 0), cc = j >= 7 ? j - 7 : j;
  const int bm = w & 1, bn = w >> 1;
  u32 ha, wa2;
  {
    const int q4 = (lane & 15) >> 2, pp = lane & 3, blk = (lane >> 4) & 1;
    ha = bm * 8192 + lane * 16;
    wa2 = (4 * hh + q4) * 128 + (4 * (bn ^ (q4 >> 1)) + 2 * blk + (pp >> 1)) * 16 + 8 * (pp & 1);
  }
  f16v ag, accb;
#pragma unroll
  for (int r = 0; r < 16; ++r) { accb[r] = 0.f; ag[r] = 0.f; }
  int g = 0;
#pragma unroll 1
  for (int seg = 0; seg < 3; ++seg) {
    const int gend = helper ? 0 : (seg == 0 ? 7 : (seg == 1 ? 14 : 18));
#pragma unroll 1
    for (; g < gend; ++g) {
      bar();
      LAS const char* hp = lds + hslot(g) + ha;
      LAS const char* wp = lds + wslot(g) + wa2;
      bf8 fh[2], fw[2];
      fh[0] = lds_frag(hp);
      fw[0] = lds_tr2(wp, wp + 1024);
#pragma unroll
      for (int ks = 0; ks < 8; ++ks) {
        if (ks < 7) {
          fh[(ks + 1) & 1] = lds_frag(hp + (ks + 1) * 1024);
          fw[(ks + 1) & 1] = lds_tr2(wp + (ks + 1) * 2048, wp + (ks + 1) * 2048 + 1024);
        }
        accb = mfma32(fw[ks & 1], fh[ks & 1], accb);
        SCHED();
      }
    }
    if (seg == 0) {
      if (helper) {
        const int sign = j - 14;
        flag_wait(p.ws, FL_KV + ((i * 2 + sign) * 16 + lane) * FLS, 16, tag, lane, 5);
        STAMP(12);
        f4v sum = {0.f, 0.f, 0.f, 0.f};
        const u32 o = OFF_KVP + (i * 2 + sign) * 16 * 4096 + w * 1024 + lane * 16;
        u4v t[16];
#pragma unroll
        for (int q = 0; q < 16; ++q) t[q] = ld128(rs, o + q * 4096);
#pragma unroll
        for (int q = 0; q < 16; ++q)
#pragma unroll
          for (int e = 0; e < 4; ++e) sum[e] += __uint_as_float(t[q][e]);
        const bool fvalid = !(w == 3 && hh == 1);
#pragma unroll
        for (int e = 0; e < 4; ++e) sum[e] = !fvalid || l31 >= 30 ? 0.f : (l31 == 29 ? 1.f : sum[e]);
        st64(rs, OFF_KVT + ((i * 2 + sign) * 2 + (w >> 1)) * 1024 + lane * 16 + 8 * (w & 1), u2v{pk2(sum[0], sum[1]), pk2(sum[2], sum[3])});
        flag_set(p.ws, FL_KR + ((i * 2 + sign) * 4 + w) * FLS, tag, lane);
        STAMP(13);
      }
      flag_wait(p.ws, FL_KR + (j * 8 + lane) * FLS, 8, tag, lane, 6);
      STAMP(14);
      f16v np, nn;
#pragma unroll
      for (int r = 0; r < 16; ++r) { np[r] = 0.f; nn[r] = 0.f; }
      u4v kt[4];
#pragma unroll
      for (int q = 0; q < 4; ++q) kt[q] = ld128(rs, OFF_KVT + (j * 4 + q) * 1024 + lane * 16);
      np = mfma32(as_bf8(kt[0]), eq[0][0], np);
      nn = mfma32(as_bf8(kt[2]), eq[1][0], nn);
      np = mfma32(as_bf8(kt[1]), eq[0][1], np);
      nn = mfma32(as_bf8(kt[3]), eq[1][1], nn);
      const float ia = __builtin_amdgcn_rcpf(xhalf_hi(np[13])), ib = __builtin_amdgcn_rcpf(xhalf_hi(nn[13]));
      const float den = xhalf_hi(np[12]) * ia + xhalf_hi(nn[12]) * ib + 1e-6f;
      const float id = __builtin_amdgcn_rcpf(den), fa = ia * id, fb = ib * id;
#pragma unroll
      for (int r = 0; r < 16; ++r) {
        const float o = np[r] * fa + nn[r] * fb;
        np[r] = (r >= 12 && hh) ? 0.f : o;
      }
      const u32 ob = OFF_O + ((4 * i + w) * 32 + 2 * j) * 1024 + lane * 16;
      st128(rs, ob, __builtin_bit_cast(u4v, frag8(np, 0)));
      st128(rs, ob + 1024, __builtin_bit_cast(u4v, frag8(np, 1)));
      flag_set(p.ws, FL_O + ((i * 16 + j) * 4 + w) * FLS, tag, lane);
      STAMP(15);
      if (helper) return;
    } else if (seg == 1) {
      ag = accb;
#pragma unroll
      for (int r = 0; r < 16; ++r) accb[r] = 0.f;
    }
  }
  STAMP(16);
  {
    float m2, s2;
    stat_wait(p.ws, rs, 64 * m64 + 32 * bm + l31, tag, lane, m2, s2);
    const float var = s2 * s2;
    u32 vh, vl;
    split2(var, vh, vl);
    u4v xa2 = {vh | (vl << 16), 0u, 0u, 0u};
    const u32 b2 = *((const GAS u16*)p.b2 + cc * 64 + bn * 32 + l31);
    u4v wa3 = {b2 | (b2 << 16), 0u, 0u, 0u};
    if (hh) { xa2[0] = 0u; wa3[0] = 0u; }
    ag = mfma32(as_bf8(wa3), as_bf8(xa2), ag);
    const float r2 = __builtin_amdgcn_rcpf(var);
    const int row = 32 * bm + l31;
    LAS char* base = lds + L_V + row * 128 + 8 * hh;
    const int key = (row >> 1) & 7;
#pragma unroll
    for (int q = 0; q < 4; ++q) {
      LAS u2v* ptr = (LAS u2v*)(base + (((4 * bn + q) ^ key) << 4));
      const u2v xv = *ptr;
      float o[4];
#pragma unroll
      for (int e = 0; e < 4; ++e) {
        const float gp = ag[4 * q + e] * r2;
        const float sg = gp * __builtin_amdgcn_rcpf(1.f + __builtin_amdgcn_exp2f(-gp * LOG2E));
        const float xr = (e & 1) ? bf_hi(xv[e >> 1]) : bf_lo(xv[e >> 1]);
        o[e] = __builtin_fmaf(accb[4 * q + e], sg, xr);
      }
      *ptr = u2v{pk2(o[0], o[1]), pk2(o[2], o[3])};
    }
  }
  bar();
  {
    const int row = 8 * wave + (lane >> 3);
    u4v v = *(LAS const u4v*)(lds + L_V + row * 128 + (((lane & 7) ^ ((row >> 1) & 7)) << 4));
    *(GAS u4v*)((char*)p.out + (size_t)(64 * m64 + row) * XROW + cc * 128 + (lane & 7) * 16) = v;
  }
  STAMP(17);
}

__global__ __launch_bounds__(512, 2) void fused_kernel(Params p) {
  extern __shared__ __attribute__((aligned(16))) char smem[];
  LAS char* lds = (LAS char*)smem;
  const int lane = threadIdx.x & 63;
  const int wave = __builtin_amdgcn_readfirstlane(threadIdx.x >> 6);
  const int b = blockIdx.x, chi = b & 7, sidx = b >> 3;
  const int i = 2 * chi + (sidx >> 4), j = sidx & 15;
  const bool helper = j >= 14;
  STAMP(0);
  if (wave >= 4) loader_main(p, (u32)(size_t)lds, i, j, helper, wave - 4, lane, wave);
  else compute_main(p, lds, i, j, helper, wave, lane, wave);
}

extern "C" void kernel_launch(void* const* d_in, const int* in_sizes, int n_in, void* d_out, int out_size, void* d_ws,
                              size_t ws_size, hipStream_t stream) {
  Params p;
  p.x = (const u16*)d_in[0];
  p.wqkv = (const u16*)d_in[1];
  p.wphi = (const u16*)d_in[2];
  p.wo = (const u16*)d_in[3];
  p.w1 = (const u16*)d_in[5];
  p.b1 = (const u16*)d_in[6];
  p.w2 = (const u16*)d_in[7];
  p.b2 = (const u16*)d_in[8];
  p.out = (u16*)d_out;
  p.ws = (char*)d_ws;
  (void)hipFuncSetAttribute((const void*)fused_kernel, hipFuncAttributeMaxDynamicSharedMemorySize, LDS_BYTES);
  fused_kernel<<<256, 512, LDS_BYTES, stream>>>(p);
}
```

```cpp
#include <hip/hip_runtime.h>
#include <hip/hip_bf16.h>
#include <stdint.h>

typedef unsigned short u16;
typedef uint32_t u32;
typedef uint64_t u64;
typedef __bf16 bf2 __attribute__((ext_vector_type(2)));
typedef __bf16 bf4 __attribute__((ext_vector_type(4)));
typedef __bf16 bf8 __attribute__((ext_vector_type(8)));
typedef float f4v __attribute__((ext_vector_type(4)));
typedef float f16v __attribute__((ext_vector_type(16)));
typedef u32 u2v __attribute__((ext_vector_type(2)));
typedef u32 u4v __attribute__((ext_vector_type(4)));
#define LAS __attribute__((address_space(3)))
#define GAS __attribute__((address_space(1)))
#define CAS __attribute__((address_space(4)))
#define DI __device__ __forceinline__

constexpr int S_ = 2048, D_ = 448, NH = 16, DH = 28, FF = 1792;
constexpr int XROW = D_ * 2;
constexpr int QROW = 3 * D_ * 2;
constexpr int W1ROW = FF * 2;

constexpr u32 WS_EPOCH = 0, WS_ERR = 128;
constexpr u32 FL0 = 4096, FLS = 128;
constexpr u32 FL_P0 = FL0;
constexpr u32 FL_C1 = FL_P0 + 768 * FLS;
constexpr u32 FL_H = FL_C1 + 1024 * FLS;
constexpr u32 FL_KV = FL_H + 1024 * FLS;
constexpr u32 FL_KR = FL_KV + 512 * FLS;
constexpr u32 FL_O = FL_KR + 128 * FLS;
constexpr u32 OFF_ST = FL_O + 1024 * FLS;
constexpr u32 OFF_WP = OFF_ST + S_ * 16;
constexpr u32 WP_HEAD = 84 * 1024, WP_Q = 56 * 1024;
constexpr u32 OFF_CSP = OFF_WP + NH * WP_HEAD;
constexpr u32 OFF_C1P = OFF_CSP + NH * 3 * 2048;
constexpr u32 OFF_H = OFF_C1P + 256 * 512;
constexpr u32 OFF_KVP = OFF_H + 64 * 112 * 1024;
constexpr u32 OFF_KVT = OFF_KVP + NH * 2 * 16 * 4096;
constexpr u32 OFF_O = OFF_KVT + NH * 4 * 1024;
constexpr u32 OFF_DIAG = OFF_O + 64 * 32 * 1024;
constexpr u32 WS_USED = OFF_DIAG + 256 * 8 * 64 * 8;

constexpr u32 SLOT = 16384;
constexpr u32 L_PHI = 8 * SLOT;
constexpr u32 L_V = L_PHI + 16384;
constexpr u32 L_AUG = L_V + 8192;
constexpr u32 L_DUMMY = L_AUG + 7168 + 512;
constexpr u32 LDS_BYTES = 163840;

constexpr float LOG2E = 1.4426950408889634f;
constexpr u32 SPIN_CAP = 1u << 18;

struct Params {
  const u16 *x, *wqkv, *wphi, *wo, *w1, *b1, *w2, *b2;
  u16* out;
  char* ws;
};

#define STAMP(id)
#define STAMPC(id)
#define DIAG_INIT()
#define DIAG_DUMP()

DI float bf_lo(u32 u) { return __uint_as_float(u << 16); }
DI float bf_hi(u32 u) { return __uint_as_float(u & 0xffff0000u); }
DI u32 pk2(float a, float b) {
  bf2 v = {(__bf16)a, (__bf16)b};
  return __builtin_bit_cast(u32, v);
}
DI u32 bf_rn(float a) { return pk2(a, 0.f) & 0xffffu; }
DI bf8 as_bf8(u4v v) { return __builtin_bit_cast(bf8, v); }
DI bf8 frag8(const f16v& a, int s) {
  u4v r = {pk2(a[8 * s], a[8 * s + 1]), pk2(a[8 * s + 2], a[8 * s + 3]), pk2(a[8 * s + 4], a[8 * s + 5]), pk2(a[8 * s + 6], a[8 * s + 7])};
  return as_bf8(r);
}
DI f16v mfma32(bf8 a, bf8 b, f16v c) { return __builtin_amdgcn_mfma_f32_32x32x16_bf16(a, b, c, 0, 0, 0); }
DI f4v mfma16(bf8 a, bf8 b, f4v c) { return __builtin_amdgcn_mfma_f32_16x16x32_bf16(a, b, c, 0, 0, 0); }
DI bf8 lds_frag(LAS const char* p) { return *(LAS const bf8*)p; }
DI bf8 lds_tr2(LAS const char* p0, LAS const char* p1) {
  bf4 a = __builtin_amdgcn_ds_read_tr16_b64_v4bf16((LAS bf4*)p0);
  bf4 b = __builtin_amdgcn_ds_read_tr16_b64_v4bf16((LAS bf4*)p1);
  return __builtin_shufflevector(a, b, 0, 1, 2, 3, 4, 5, 6, 7);
}
DI float xhalf_max(float v) {
  auto r = __builtin_amdgcn_permlane32_swap(__float_as_uint(v), __float_as_uint(v), false, false);
  return __builtin_elementwise_maximum(__uint_as_float(r[0]), __uint_as_float(r[1]));
}
DI float xhalf_min(float v) {
  auto r = __builtin_amdgcn_permlane32_swap(__float_as_uint(v), __float_as_uint(v), false, false);
  return __builtin_elementwise_minimum(__uint_as_float(r[0]), __uint_as_float(r[1]));
}
DI float xhalf_sum(float v) {
  auto r = __builtin_amdgcn_permlane32_swap(__float_as_uint(v), __float_as_uint(v), false, false);
  return __uint_as_float(r[0]) + __uint_as_float(r[1]);
}
DI float xhalf_hi(float v) {
  auto r = __builtin_amdgcn_permlane32_swap(__float_as_uint(v), __float_as_uint(v), false, false);
  return __uint_as_float(r[1]);
}
DI float dpp_ror(float v, int n) {
  const int iv = __builtin_bit_cast(int, v);
  int r;
  switch (n) {
    case 8: r = __builtin_amdgcn_update_dpp(0, iv, 0x128, 0xf, 0xf, false); break;
    case 4: r = __builtin_amdgcn_update_dpp(0, iv, 0x124, 0xf, 0xf, false); break;
    case 2: r = __builtin_amdgcn_update_dpp(0, iv, 0x122, 0xf, 0xf, false); break;
    default: r = __builtin_amdgcn_update_dpp(0, iv, 0x121, 0xf, 0xf, false); break;
  }
  return __builtin_bit_cast(float, r);
}
DI float half_sum(float v) {
  v += dpp_ror(v, 8); v += dpp_ror(v, 4); v += dpp_ror(v, 2); v += dpp_ror(v, 1);
  auto r = __builtin_amdgcn_permlane16_swap(__float_as_uint(v), __float_as_uint(v), false, false);
  return __uint_as_float(r[0]) + __uint_as_float(r[1]);
}
typedef float f2v __attribute__((ext_vector_type(2)));
DI f2v pair(const f16v& a, int r) { return f2v{a[r], a[r + 1]}; }
DI u32 pk2v(f2v v) { return pk2(v[0], v[1]); }
DI void bar() { asm volatile("s_waitcnt lgkmcnt(0)\n\ts_barrier" ::: "memory"); }
DI void bar_only() { asm volatile("s_barrier" ::: "memory"); }
template <int N> DI void vmwait() { asm volatile("s_waitcnt vmcnt(%0)" ::"i"(N) : "memory"); }
#define SCHED() __builtin_amdgcn_sched_barrier(0)

DI void dma(u32 voff, const void* sbase, u32 ldsaddr) {
  asm volatile("s_mov_b32 m0, %2\n\ts_nop 0\n\tglobal_load_lds_dwordx4 %0, %1" ::"v"(voff), "s"(sbase), "s"(ldsaddr) : "memory");
}
DI void dma_sc1(u32 voff, const void* sbase, u32 ldsaddr) {
  asm volatile("s_mov_b32 m0, %2\n\ts_nop 0\n\tglobal_load_lds_dwordx4 %0, %1 sc1" ::"v"(voff), "s"(sbase), "s"(ldsaddr) : "memory");
}

DI void dma4(u32 v0, u32 v1, u32 v2, u32 v3, const void* sbase, u32 ldsaddr) {
  asm volatile("s_mov_b32 m0, %5\n\ts_nop 0\n\tglobal_load_lds_dwordx4 %0, %4\n\tglobal_load_lds_dwordx4 %1, %4 offset:1024\n\t"
               "global_load_lds_dwordx4 %2, %4 offset:2048\n\tglobal_load_lds_dwordx4 %3, %4 offset:3072"
               ::"v"(v0), "v"(v1), "v"(v2), "v"(v3), "s"(sbase), "s"(ldsaddr) : "memory");
}
template <int N> DI void dma_lin(u32 v, const void* sbase, u32 ldsaddr) {
  if constexpr (N == 1)
    asm volatile("s_mov_b32 m0, %2\n\ts_nop 0\n\tglobal_load_lds_dwordx4 %0, %1 sc1" ::"v"(v), "s"(sbase), "s"(ldsaddr) : "memory");
  else if constexpr (N == 2)
    asm volatile("s_mov_b32 m0, %2\n\ts_nop 0\n\tglobal_load_lds_dwordx4 %0, %1 sc1\n\tglobal_load_lds_dwordx4 %0, %1 offset:1024 sc1"
                 ::"v"(v), "s"(sbase), "s"(ldsaddr) : "memory");
  else
    asm volatile("s_mov_b32 m0, %2\n\ts_nop 0\n\tglobal_load_lds_dwordx4 %0, %1 sc1\n\tglobal_load_lds_dwordx4 %0, %1 offset:1024 sc1\n\t"
                 "global_load_lds_dwordx4 %0, %1 offset:2048 sc1\n\tglobal_load_lds_dwordx4 %0, %1 offset:3072 sc1"
                 ::"v"(v), "s"(sbase), "s"(ldsaddr) : "memory");
}

typedef __amdgpu_buffer_rsrc_t rsrc_t;
DI u4v ld128(rsrc_t r, u32 voff, u32 soff) { return __builtin_amdgcn_raw_buffer_load_b128(r, voff, soff, 16); }
DI u32 ld32(rsrc_t r, u32 voff, u32 soff) { return __builtin_amdgcn_raw_buffer_load_b32(r, voff, soff, 16); }
DI void st128(rsrc_t r, u32 voff, u32 soff, u4v v) { __builtin_amdgcn_raw_buffer_store_b128(v, r, voff, soff, 16); }
DI void st64(rsrc_t r, u32 voff, u32 soff, u2v v) { __builtin_amdgcn_raw_buffer_store_b64(v, r, voff, soff, 16); }
DI void st32(rsrc_t r, u32 voff, u32 soff, u32 v) { __builtin_amdgcn_raw_buffer_store_b32(v, r, voff, soff, 16); }
DI float fmx(float a, float b) { return __builtin_elementwise_maximum(a, b); }
DI float fmn(float a, float b) { return __builtin_elementwise_minimum(a, b); }
DI void touch(u32 voff, const void* sbase, u32 lb) {
  asm volatile("s_mov_b32 m0, %2\n\ts_nop 0\n\tglobal_load_lds_dword %0, %1" ::"v"(voff), "s"(sbase), "s"(lb + L_DUMMY) : "memory");
}

DI u64 tag64(u32 t) { return (u64)t | ((u64)(t * 0x9E3779B1u + 0x7F4A7C15u) << 32); }

DI void flag_set(char* ws, u32 off, u64 tag, int lane) {
  asm volatile("s_waitcnt vmcnt(0)" ::: "memory");
  if (lane == 0) __hip_atomic_store((GAS u64*)(ws + off), tag, __ATOMIC_RELAXED, __HIP_MEMORY_SCOPE_AGENT);
}
DI void flag_wait(char* ws, u32 off, int n, u64 tag, int lane, u32 code) {
  GAS u64* ptr = (GAS u64*)(ws + off);
#pragma unroll 1
  for (u32 spin = 0;; ++spin) {
    u64 v = tag;
    if (lane < n) v = __hip_atomic_load(ptr, __ATOMIC_RELAXED, __HIP_MEMORY_SCOPE_AGENT);
    if (__all(v == tag)) break;
    if (spin > SPIN_CAP) {
      if (lane == 0) __hip_atomic_store((GAS u32*)(ws + WS_ERR), code, __ATOMIC_RELAXED, __HIP_MEMORY_SCOPE_AGENT);
      break;
    }
    __builtin_amdgcn_s_sleep(1);
  }
  asm volatile("" ::: "memory");
}
DI void stat_wait(char* ws, rsrc_t rs, int token, u64 tag, int lane, float& mean, float& sigma) {
#pragma unroll 1
  for (u32 spin = 0;; ++spin) {
    u4v g = ld128(rs, token * 16, OFF_ST);
    mean = __uint_as_float(g[0]);
    sigma = __uint_as_float(g[1]);
    bool ok = g[2] == (u32)tag && g[3] == (u32)(tag >> 32);
    if (__all(ok)) break;
    if (spin > SPIN_CAP) {
      if (lane == 0) __hip_atomic_store((GAS u32*)(ws + WS_ERR), 99u, __ATOMIC_RELAXED, __HIP_MEMORY_SCOPE_AGENT);
      break;
    }
    __builtin_amdgcn_s_sleep(1);
  }
}
DI void split2(float v, u32& hi, u32& lo) {
  hi = bf_rn(v);
  lo = bf_rn(v - __uint_as_float(hi << 16));
}
DI bf8 aug_w(float cs, u32 beta, int hh) {
  u32 hi, lo;
  split2(cs, hi, lo);
  u4v r = {hi | (hi << 16), lo | (beta << 16), beta, 0u};
  if (hh) r = u4v{0u, 0u, 0u, 0u};
  return as_bf8(r);
}

struct Role {
  int i, j;
  int jw;
  int m64, cc;
  int tq;
  bool bwork;
  int rho;
  int sidx;
};

DI u32 wslot(int g) { return (u32)((0x1076u >> (4 * (g & 3))) & 15) * SLOT; }
DI u32 hslot(int g) { return (u32)((0x3254u >> (4 * (g & 3))) & 15) * SLOT; }
DI constexpr u32 fslot(int c) { return (u32)(2 * (c % 4)) * SLOT; }
constexpr u32 L_KVA = 6 * SLOT;
constexpr u32 L_KVB = 0;
constexpr u32 L_Q = 2 * SLOT;
constexpr u32 L_HS = 4 * SLOT;

DI void loader_main(const Params& p, u32 lb, const Role& R, int lw, int lane, int wave) {
  const int i = R.i, j = R.j, m64 = R.m64, cc = R.cc;
  const u32 vx0 = (lane >> 3) * XROW + (((lane & 7) ^ (lane >> 4)) << 4);
  const u32 vx1 = (vx0 ^ 64) + (8 * XROW - 1024), vx2 = vx0 + 2 * (8 * XROW - 1024), vx3 = vx1 + 2 * (8 * XROW - 1024);
  const u32 vw0 = (lane >> 4) * W1ROW + (((lane & 15) ^ ((lane >> 4) << 2)) << 4);
  const u32 vw1 = vw0 + (4 * W1ROW - 1024), vw2 = vw0 + 2 * (4 * W1ROW - 1024), vw3 = vw0 + 3 * (4 * W1ROW - 1024);
  const char* xb = (const char*)p.x + (size_t)(128 * i + 32 * lw) * XROW;
  const char* w1b = (const char*)p.w1 + (size_t)(16 * lw) * W1ROW + R.jw * 256;
  const u32 lq = lb + lw * 4096;
  int gc = 0;
  auto stage = [&](u32 slot) {
    dma4(vx0, vx1, vx2, vx3, xb + gc * 128, lq + slot);
    dma4(vw0, vw1, vw2, vw3, w1b + (size_t)gc * (64 * W1ROW), lq + slot + SLOT);
    ++gc;
  };
  bar_only();
  STAMP(12);
  {
    const char* b = (const char*)p.x + (size_t)(128 * i + 8 * j + 2 * lw) * XROW;
    dma(min(lane, 55) * 16, b, lb + L_V + lw * 2048);
    dma(min(lane, 55) * 16, b + XROW, lb + L_V + lw * 2048 + 1024);
  }
  stage(fslot(0));
  STAMP(13);
  {
    const int lx = min(lane, 47), lz = min(lane, 23);
    touch((32 * (j & 3) + 8 * lw + (lx & 7)) * XROW + (1 + (lx >> 3)) * 128, (const char*)p.x + (size_t)(128 * i) * XROW, lb);
    touch((64 * (1 + (lz >> 2)) + 8 * (i & 7) + 2 * lw + ((lz & 3) >> 1)) * W1ROW + (lz & 1) * 128, (const char*)p.w1 + R.jw * 256, lb);
  }
  stage(fslot(1));
  const u32 E = *(CAS const u32*)(p.ws + WS_EPOCH);
  const u64 tag = tag64(E + 1);
  const rsrc_t rs = __builtin_amdgcn_make_buffer_rsrc(p.ws, 0, WS_USED, 0x00020000);
  const u32 vl = lane * 16;
  const u32 bchunk = ((lane & 7) ^ (((lane >> 4) & 1) << 2)) << 4;
  const u32 vb0 = (lane >> 3) * XROW + bchunk;
  const u32 vb1 = vb0 + (8 * XROW - 1024), vb2 = vb0 + 2 * (8 * XROW - 1024), vb3 = vb0 + 3 * (8 * XROW - 1024);
  const u32 vb3c = min(lane >> 3, 3) * XROW + bchunk + 3 * (8 * XROW - 1024);
  vmwait<18>();
  {
    const int hh = lane >> 5, l31 = lane & 31;
    LAS const u32* src = (LAS const u32*)(size_t)(lb + L_V + lw * 2048 + hh * 1024 + l31 * 4);
    u32 xs7[7];
#pragma unroll
    for (int m = 0; m < 7; ++m) xs7[m] = src[32 * m];
    float s = 0.f;
#pragma unroll
    for (int m = 0; m < 7; ++m) s += bf_lo(xs7[m]) + bf_hi(xs7[m]);
    s = half_sum(s);
    const float mean = s * (1.f / D_);
    float q = 0.f;
#pragma unroll
    for (int m = 0; m < 7; ++m) {
      float a = bf_lo(xs7[m]) - mean, b = bf_hi(xs7[m]) - mean;
      q += a * a + b * b;
    }
    q = half_sum(q);
    const float sigma = __builtin_sqrtf(q * (1.f / D_) + 1e-5f);
    if (l31 == 0) st128(rs, (128 * i + 8 * j + 2 * lw + hh) * 16, OFF_ST, u4v{__float_as_uint(mean), __float_as_uint(sigma), (u32)tag, (u32)(tag >> 32)});
  }
  STAMP(1);
  vmwait<11>(); bar_only();
  STAMP(2);
  stage(fslot(2)); vmwait<8>(); bar_only();
  stage(fslot(3)); vmwait<8>(); bar_only();
  stage(fslot(4)); vmwait<8>(); bar_only();
  stage(fslot(5)); vmwait<8>(); bar_only();
  stage(fslot(6));
  STAMP(3);
  const char* wp = p.ws + OFF_WP + (size_t)j * WP_HEAD;
  {
    const u32 o = lane < 42 ? FL_P0 + (j * 48 + (lane / 14) * 16 + lane % 14) * FLS : FL_C1 + (R.jw * 16 + min(lane - 42, 15)) * FLS;
    flag_wait(p.ws, o, 58, tag, lane, 1);
  }
  STAMP(4);
  bar_only();
#pragma unroll
  for (int c = 0; c < 4; ++c) dma_lin<2>(vl, wp + c * 8192 + lw * 2048, lb + L_KVA + c * 8192 + lw * 2048);
  bar_only();
#pragma unroll
  for (int c = 4; c < 7; ++c) dma_lin<2>(vl, wp + c * 8192 + lw * 2048, lb + L_KVB + (c - 4) * 8192 + lw * 2048);
  bar_only();
  STAMP(5);
#pragma unroll
  for (int c = 0; c < 7; ++c) dma_lin<1>(vl, wp + WP_Q + c * 4096 + lw * 1024, lb + L_Q + c * 4096 + lw * 1024);
  if (R.bwork) {
#pragma unroll 1
    for (int q = lw; q < 9; q += 4) {
      if (q < 7) touch(lane * XROW, (const char*)p.w2 + (size_t)(448 * R.tq + 64 * q) * XROW + cc * 128, lb);
      else touch(min(lane, q == 7 ? 63 : 47) * XROW, (const char*)p.wo + (size_t)(112 * R.tq + 64 * (q - 7)) * XROW + cc * 128, lb);
    }
    if (lw == 0) vmwait<10>(); else vmwait<9>();
  } else {
    vmwait<7>();
  }
  bar_only();
  if (j < 14) {
    LAS const char* src = (LAS const char*)(size_t)(lb + L_HS + lw * 8192) + vl;
    const u32 hb = OFF_H + ((4 * i + lw) * 112 + 8 * j) * 1024;
    u4v hv[8];
#pragma unroll
    for (int q = 0; q < 8; ++q) hv[q] = *(LAS const u4v*)(src + q * 1024);
#pragma unroll
    for (int q = 0; q < 8; ++q) st128(rs, vl, hb + q * 1024, hv[q]);
  }
  STAMP(14);
  bar_only();
  STAMP(6);
  const char* w2p = (const char*)p.w2 + (size_t)(32 * lw) * XROW + cc * 128;
  auto issue_w = [&](int g) {
    if (g < 14) {
      dma4(vb0, vb1, vb2, vb3, w2p, lq + wslot(g));
      w2p += 128 * XROW;
    } else {
      dma4(vb0, vb1, vb2, vb3c, (const char*)p.wo + (size_t)(28 * (4 * (g - 14) + lw)) * XROW + cc * 128, lq + wslot(g));
    }
  };
  const char* hp = p.ws + OFF_H + (size_t)((2 * m64 + (lw >> 1)) * 112 + (lw & 1) * 4) * 1024;
  auto issue_h = [&](int g) {
    if (g == 14) hp = p.ws + OFF_O + (size_t)((2 * m64 + (lw >> 1)) * 32 + (lw & 1) * 4) * 1024;
    dma_lin<4>(vl, hp, lq + hslot(g));
    hp += 8192;
  };
  const u32 hflag = FL_H + ((i * 16 + j) * 4 + lw) * FLS;
  if (R.bwork) {
    issue_w(0);
    issue_w(1);
    if (j < 14) {
      vmwait<8>();
      if (lane == 0) __hip_atomic_store((GAS u64*)(p.ws + hflag), tag, __ATOMIC_RELAXED, __HIP_MEMORY_SCOPE_AGENT);
    }
    issue_w(2);
    issue_w(3);
    vmwait<16>();
  } else {
    vmwait<0>();
    if (j < 14 && lane == 0) __hip_atomic_store((GAS u64*)(p.ws + hflag), tag, __ATOMIC_RELAXED, __HIP_MEMORY_SCOPE_AGENT);
  }
  bar_only();
  if (R.bwork) {
    flag_wait(p.ws, FL_H + (((m64 >> 1) * 16 + (lane >> 1)) * 4 + 2 * (m64 & 1) + (lane & 1)) * FLS, 28, tag, lane, 2);
    STAMP(7);
    issue_h(0);
    issue_h(1);
    touch(lane * 128, p.ws + OFF_H + (size_t)((2 * m64 + (lw >> 1)) * 112 + 16 * cc + 8 * (lw & 1)) * 1024, lb);
  }
  bar_only();
  if (!R.bwork) return;
  issue_h(2);
  issue_h(3);
  {
    const char* b = (const char*)p.x + (size_t)(64 * m64 + 16 * lw) * XROW + cc * 128;
    dma(vx0, b, lb + L_V + lw * 2048);
    dma(vx0 ^ 64, b + 8 * XROW, lb + L_V + lw * 2048 + 1024);
  }
  STAMP(8);
  vmwait<15>(); bar_only();
  vmwait<10>(); bar_only();
  vmwait<6>(); bar_only();
#pragma unroll 1
  for (int g = 2; g < 17; ++g) {
    if (g == 12) {
      flag_wait(p.ws, FL_O + (((m64 >> 1) * 16 + (lane >> 1)) * 4 + 2 * (m64 & 1) + (lane & 1)) * FLS, 32, tag, lane, 3);
      STAMP(9);
      const int line = min((4 * cc + lw) * 19 + min(lane, 18), 511);
      touch(line * 128, p.ws + OFF_O + (size_t)(2 * m64) * 32 * 1024, lb);
    }
    if (g + 2 < 18) {
      issue_h(g + 2);
      issue_w(g + 2);
    }
    if (g == 2) vmwait<8>();
    else if (g < 16) vmwait<8>();
    else vmwait<0>();
    bar_only();
  }
  bar_only();
  STAMP(10);
  bar_only();
  {
    LAS const char* lds = (LAS const char*)(size_t)lb;
    const int row = 8 * wave + (lane >> 3);
    u4v v = *(LAS const u4v*)(lds + L_V + row * 128 + (((lane & 7) ^ ((row >> 1) & 7)) << 4));
    *(GAS u4v*)((char*)p.out + (size_t)(64 * m64 + row) * XROW + cc * 128 + (lane & 7) * 16) = v;
  }
  if (blockIdx.x == 0 && lw == 0 && lane == 0)
    __hip_atomic_store((GAS u32*)(p.ws + WS_EPOCH), E + 1, __ATOMIC_RELAXED, __HIP_MEMORY_SCOPE_AGENT);
  STAMP(11);
}

DI void exp_pair(const f16v& a, float c, f16v& ep, f16v& en) {
  float mx = fmx(a[0], a[1]), mn = fmn(a[0], a[1]);
#pragma unroll
  for (int r = 2; r < 16; r += 2) {
    mx = fmx(fmx(mx, a[r]), a[r + 1]);
    mn = fmn(fmn(mn, a[r]), a[r + 1]);
  }
  mx = xhalf_max(mx);
  mn = xhalf_min(mn);
  const f2v c2 = {c, c}, bp = {-mx * c, -mx * c}, bn = {mn * c, mn * c};
#pragma unroll
  for (int r = 0; r < 16; r += 2) {
    const f2v tp = __builtin_elementwise_fma(pair(a, r), c2, bp), tn = __builtin_elementwise_fma(pair(a, r), -c2, bn);
    ep[r] = __builtin_amdgcn_exp2f(tp[0]); ep[r + 1] = __builtin_amdgcn_exp2f(tp[1]);
    en[r] = __builtin_amdgcn_exp2f(tn[0]); en[r + 1] = __builtin_amdgcn_exp2f(tn[1]);
  }
}
DI float sum28(const f16v& e, int hh) {
  const f2v s0 = (pair(e, 0) + pair(e, 2)) + (pair(e, 4) + pair(e, 6)), s1 = pair(e, 8) + pair(e, 10), s2 = pair(e, 12) + pair(e, 14);
  const f2v t = s0 + s1;
  return xhalf_sum((t[0] + t[1]) + (hh ? 0.f : s2[0] + s2[1]));
}

DI void fold_load(const Params& p, u32 lb, int i, int head, int part, int lane, u4v (&a)[2]) {
  const int r16 = lane & 15, dg = lane >> 4;
  const int col = part == 0 ? XROW : (part == 1 ? 2 * XROW : 0);
  const char* src = (const char*)p.wqkv + (size_t)(32 * i + r16) * QROW + col + head * 56 + (dg == 3 ? 40 : dg * 16);
  a[0] = *(GAS const u4v*)src;
  a[1] = *(GAS const u4v*)(src + 16 * QROW);
  const char* wp = (const char*)p.wphi + head * 1568;
  const u32 dst = lb + L_PHI + part * 2048;
  dma(lane * 16, wp, dst);
  dma(min(lane, 33) * 16, wp + 1024, dst + 1024);
}
DI void fold_run(rsrc_t rs, LAS const char* lds, int i, int head, int part, int lane, const u4v (&a)[2]) {
  const int dg = lane >> 4, r16 = lane & 15;
  asm volatile("s_waitcnt vmcnt(0)" ::: "memory");
  u4v bw[2];
  {
    const int q4 = r16 >> 2, pp = lane & 3;
    LAS const char* b = lds + L_PHI + part * 2048 + (8 * dg + q4) * 56 + pp * 8;
#pragma unroll
    for (int ft = 0; ft < 2; ++ft) {
      u4v v = __builtin_bit_cast(u4v, lds_tr2(b + ft * 32, b + ft * 32 + 4 * 56));
      if (dg == 3) { v[2] = 0u; v[3] = 0u; }
      if (part == 1) {
        const int t = 16 * ft + r16 - 8 * dg;
        const u32 one = (16 * ft + r16 < 28) ? ((t & 1) ? 0x3f800000u : 0x3f80u) : 0u;
#pragma unroll
        for (int q = 0; q < 4; ++q) v[q] = (t >> 1) == q ? one : 0u;
      }
      bw[ft] = v;
    }
  }
  const u32 base = OFF_WP + head * WP_HEAD + (part == 2 ? WP_Q + 2 * i * 1024 : 2 * i * 2048 + part * 1024);
  const u32 step = part == 2 ? 1024 : 2048;
  float cs = 0.f;
#pragma unroll
  for (int t = 0; t < 2; ++t) {
    u4v av = a[t];
    if (dg == 3) av = u4v{av[2], av[3], 0u, 0u};
    const f4v z = {0.f, 0.f, 0.f, 0.f};
    f4v d0 = mfma16(as_bf8(av), as_bf8(bw[0]), z), d1 = mfma16(as_bf8(av), as_bf8(bw[1]), z);
    if (part != 1) {
#pragma unroll
      for (int r = 0; r < 4; ++r) {
        const int v = __float_as_int(d1[r]);
        d1[r] = __int_as_float(__builtin_amdgcn_update_dpp(v, v, 0x15B, 0xf, 0x8, false));
      }
    }
    auto s0 = __builtin_amdgcn_permlane16_swap(pk2(d0[0], d0[1]), pk2(d1[0], d1[1]), false, false);
    auto s1 = __builtin_amdgcn_permlane16_swap(pk2(d0[2], d0[3]), pk2(d1[2], d1[3]), false, false);
    const u4v o = {s0[0], s1[0], s0[1], s1[1]};
    st128(rs, lane * 16, base + t * step, o);
    cs += ((bf_lo(o[0]) + bf_hi(o[0])) + (bf_lo(o[1]) + bf_hi(o[1]))) + ((bf_lo(o[2]) + bf_hi(o[2])) + (bf_lo(o[3]) + bf_hi(o[3])));
  }
  cs = xhalf_sum(cs);
  if (lane < 32) st32(rs, lane * 4, OFF_CSP + ((head * 3 + part) * 16 + i) * 128, __float_as_uint(cs));
}

constexpr int FD = 6, FNB = FD + 1;
template <int U>
DI void f_request(LAS const char* lds, u32 (&xa)[4], u32 (&wa)[4], bf8 (&x)[7][4], bf8 (&wf)[FNB]) {
  if constexpr (U < 112) {
    constexpr int c = U >> 4, s = (U >> 2) & 3, n = U & 3;
    if constexpr ((U & 31) == 0 && U > 0) {
#pragma unroll
      for (int q = 0; q < 4; ++q) {
        xa[q] ^= 65536u;
        wa[q] ^= 65536u;
        asm volatile("" : "+v"(xa[q]), "+v"(wa[q]));
      }
    }
    constexpr u32 xs = fslot(c) & 65535u, ws = xs + SLOT;
    if constexpr (n == 0) x[c][s] = lds_frag(lds + xs + xa[s]);
    wf[U % FNB] = lds_tr2(lds + ws + wa[n] + 4096 * s, lds + ws + wa[n] + 4096 * s + 1024);
  }
}
template <int C, int V>
DI void f_unit(LAS const char* lds, u32 (&xa)[4], u32 (&wa)[4], bf8 (&x)[7][4], bf8 (&wf)[FNB], f16v (&acc)[4]) {
  constexpr int U = 16 * C + V;
  if constexpr (V == 16 - FD && C < 6) bar_only();
  f_request<U + FD>(lds, xa, wa, x, wf);
  acc[V & 3] = mfma32(wf[U % FNB], x[C][V >> 2], acc[V & 3]);
  SCHED();
  if constexpr (V < 15) f_unit<C, V + 1>(lds, xa, wa, x, wf, acc);
}
template <int C>
DI void tick_f(LAS const char* lds, u32 (&xa)[4], u32 (&wa)[4], bf8 (&x)[7][4], bf8 (&wf)[FNB], f16v (&acc)[4]) {
  f_unit<C, 0>(lds, xa, wa, x, wf, acc);
}
template <int U>
DI void f_prime(LAS const char* lds, u32 (&xa)[4], u32 (&wa)[4], bf8 (&x)[7][4], bf8 (&wf)[FNB]) {
  f_request<U>(lds, xa, wa, x, wf);
  if constexpr (U + 1 < FD) f_prime<U + 1>(lds, xa, wa, x, wf);
}

DI void compute_main(const Params& p, LAS char* lds, const Role& R, int w, int lane, int wave) {
  const rsrc_t rs = __builtin_amdgcn_make_buffer_rsrc(p.ws, 0, WS_USED, 0x00020000);
  const int i = R.i, j = R.j;
  const int hh = lane >> 5, l31 = lane & 31;
  const bool ffn = j < 14;
  const bool fold = i < 14 && w < 3;
  const u32 E = *(CAS const u32*)(p.ws + WS_EPOCH);
  const u64 tag = tag64(E + 1);
  if (fold) {
    u4v fa[2];
    fold_load(p, (u32)(size_t)lds, i, j, w, lane, fa);
    SCHED();
    bar_only();
    SCHED();
    STAMP(22);
    fold_run(rs, lds, i, j, w, lane, fa);
  } else if (w < 3) {
    bar_only();
  } else {
    u4v fc[7];
    const char* src = (const char*)p.w1 + (size_t)(28 * i + (lane >> 4)) * W1ROW + R.jw * 256 + (lane & 15) * 16;
#pragma unroll
    for (int q = 0; q < 7; ++q) fc[q] = *(GAS const u4v*)(src + (size_t)q * 4 * W1ROW);
    SCHED();
    bar_only();
    SCHED();
    float cs[8];
#pragma unroll
    for (int e = 0; e < 8; ++e) cs[e] = 0.f;
#pragma unroll
    for (int q = 0; q < 7; ++q)
#pragma unroll
      for (int e = 0; e < 4; ++e) { cs[2 * e] += bf_lo(fc[q][e]); cs[2 * e + 1] += bf_hi(fc[q][e]); }
#pragma unroll
    for (int e = 0; e < 8; ++e) {
      auto r = __builtin_amdgcn_permlane16_swap(__float_as_uint(cs[e]), __float_as_uint(cs[e]), false, false);
      cs[e] = xhalf_sum(__uint_as_float(r[0]) + __uint_as_float(r[1]));
    }
    if (ffn && lane < 32) {
      const int o = (lane >> 4) * 4;
      st128(rs, (lane & 15) * 32 + (lane >> 4) * 16, OFF_C1P + (j * 16 + i) * 512,
            u4v{__float_as_uint(o ? cs[4] : cs[0]), __float_as_uint(o ? cs[5] : cs[1]), __float_as_uint(o ? cs[6] : cs[2]), __float_as_uint(o ? cs[7] : cs[3])});
    }
  }
  STAMP(23);
  STAMP(1);
  u32 xa[4], wa[4];
  {
    const int row = 32 * w + l31;
    const u32 b = row * 128 + ((hh ^ ((row >> 1) & 7)) << 4);
#pragma unroll
    for (int s = 0; s < 4; ++s) xa[s] = b ^ (32 * s);
    const int q4 = (lane & 15) >> 2, pp = lane & 3, blk = (lane >> 4) & 1;
#pragma unroll
    for (int n = 0; n < 4; ++n) {
      const int nt = (n + w) & 3;
      wa[n] = (8 * hh + q4) * 256 + (4 * (nt ^ q4) + 2 * blk + (pp >> 1)) * 16 + 8 * (pp & 1);
    }
  }
  bf8 x[7][4], wf[FNB];
  f16v acc[4];
#pragma unroll
  for (int n = 0; n < 4; ++n)
#pragma unroll
    for (int r = 0; r < 16; ++r) acc[n][r] = 0.f;

  bar();
  STAMP(2);
  f_prime<0>(lds, xa, wa, x, wf);
  tick_f<0>(lds, xa, wa, x, wf, acc);
  tick_f<1>(lds, xa, wa, x, wf, acc);
  asm volatile("s_waitcnt vmcnt(0)" ::: "memory");
  if (lane == 0) {
    if (fold) __hip_atomic_store((GAS u64*)(p.ws + FL_P0 + ((j * 3 + w) * 16 + i) * FLS), tag, __ATOMIC_RELAXED, __HIP_MEMORY_SCOPE_AGENT);
    if (ffn && w == 3) __hip_atomic_store((GAS u64*)(p.ws + FL_C1 + (j * 16 + i) * FLS), tag, __ATOMIC_RELAXED, __HIP_MEMORY_SCOPE_AGENT);
  }
  STAMP(19);
  const int token = 128 * i + 32 * w + l31;
  u4v sg = ld128(rs, token * 16, OFF_ST);
  tick_f<2>(lds, xa, wa, x, wf, acc);
  tick_f<3>(lds, xa, wa, x, wf, acc);
  STAMP(20);
  float mean = __uint_as_float(sg[0]), sigma = __uint_as_float(sg[1]);
  if (!__all(sg[2] == (u32)tag && sg[3] == (u32)(tag >> 32))) stat_wait(p.ws, rs, token, tag, lane, mean, sigma);
  STAMP(21);
  tick_f<4>(lds, xa, wa, x, wf, acc);
  u32 c1[8], cf[7];
#pragma unroll
  for (int q = 0; q < 8; ++q) c1[q] = ld32(rs, (8 * hh + q) * 512 + l31 * 4, OFF_C1P + R.jw * 16 * 512 + w * 128);
#pragma unroll
  for (int q = 0; q < 7; ++q) cf[q] = ld32(rs, (7 * hh + q) * 128 + l31 * 4, OFF_CSP + (j * 3 + min(w, 2)) * 2048);
  const u32 beta1 = *((const GAS u16*)p.b1 + R.jw * 128 + w * 32 + l31);
  bf8 xaug;
  {
    u32 mh, ml, sh, sl;
    split2(-mean, mh, ml);
    split2(sigma, sh, sl);
    u4v r = {mh | (ml << 16), mh | (sh << 16), sl, 0u};
    if (hh) r = u4v{0u, 0u, 0u, 0u};
    xaug = as_bf8(r);
  }
  const float rstd = __builtin_amdgcn_rcpf(sigma);
  tick_f<5>(lds, xa, wa, x, wf, acc);
  {
    float a = 0.f, b = 0.f;
#pragma unroll
    for (int q = 0; q < 7; ++q) { a += __uint_as_float(c1[q]); b += __uint_as_float(cf[q]); }
    a += __uint_as_float(c1[7]);
    *(LAS bf8*)(lds + L_AUG + w * 1024 + lane * 16) = aug_w(xhalf_sum(a), beta1, hh);
    if (w < 3) *(LAS bf8*)(lds + L_AUG + (4 + w) * 1024 + lane * 16) = aug_w(xhalf_sum(b), (w == 1 && l31 == 28) ? 0x3f80u : 0u, hh);
  }
  STAMP(3);
  tick_f<6>(lds, xa, wa, x, wf, acc);
  STAMP(4);
  bar();
  STAMP(6);
  const u32 vl = lane * 16;
  if (ffn) {
#pragma unroll
    for (int n = 0; n < 4; ++n) acc[n] = mfma32(lds_frag(lds + L_AUG + ((n + w) & 3) * 1024 + vl), xaug, acc[n]);
    LAS char* hb = lds + L_HS + w * 8192 + vl;
#pragma unroll
    for (int n = 0; n < 4; ++n) {
#pragma unroll
      for (int r = 0; r < 16; r += 2) {
        const f2v t = {fmx(acc[n][r], 0.f), fmx(acc[n][r + 1], 0.f)};
        const f2v u = t * t;
        acc[n][r] = u[0];
        acc[n][r + 1] = u[1];
      }
#pragma unroll
      for (int s = 0; s < 2; ++s) *(LAS bf8*)(hb + (2 * ((n + w) & 3) + s) * 1024) = frag8(acc[n], s);
    }
  }
  bar();
  STAMP(5);
  f16v ak, av;
#pragma unroll
  for (int r = 0; r < 16; ++r) { ak[r] = 0.f; av[r] = 0.f; }
  {
    LAS const char* wb = lds + vl;
    auto kvo = [](int ks) -> u32 { return ks < 16 ? L_KVA + ks * 2048 : L_KVB + (ks - 16) * 2048; };
    auto kvo2 = [&](int ks) -> u32 { return ks < 28 ? kvo(ks) : L_AUG + 4 * 1024; };
    bf8 fk[4], fv[4];
#pragma unroll
    for (int ks = 0; ks < 3; ++ks) {
      fk[ks] = lds_frag(wb + kvo(ks));
      fv[ks] = lds_frag(wb + kvo(ks) + 1024);
    }
#pragma unroll
    for (int ks = 0; ks < 29; ++ks) {
      if (ks + 3 < 29) {
        fk[(ks + 3) & 3] = lds_frag(wb + kvo2(ks + 3));
        fv[(ks + 3) & 3] = lds_frag(wb + kvo2(ks + 3) + 1024);
      }
      const bf8 xb = ks < 28 ? x[(ks < 28 ? ks : 0) >> 2][ks & 3] : xaug;
      ak = mfma32(fk[ks & 3], xb, ak);
      av = mfma32(fv[ks & 3], xb, av);
      __builtin_amdgcn_sched_group_barrier(0x8, 1, 0);
      __builtin_amdgcn_sched_group_barrier(0x100, 1, 0);
      __builtin_amdgcn_sched_group_barrier(0x8, 1, 0);
      __builtin_amdgcn_sched_group_barrier(0x100, 1, 0);
      SCHED();
    }
  }
  bar();
  STAMP(7);
  const float cexp = rstd * LOG2E;
  {
    f16v ep, en;
    exp_pair(ak, cexp, ep, en);
    const float ap = rstd * __builtin_amdgcn_rcpf(sum28(ep, hh)), an = rstd * __builtin_amdgcn_rcpf(sum28(en, hh));
    const int row = 32 * w + l31;
    LAS char* base = lds + row * 64 + 8 * hh;
    const int key = (l31 >> 1) & 3;
#pragma unroll
    for (int q = 0; q < 4; ++q) {
      const u32 o = (q ^ key) << 4;
      *(LAS u2v*)(base + L_PHI + o) = u2v{pk2v(pair(ep, 4 * q) * ap), pk2v(pair(ep, 4 * q + 2) * ap)};
      *(LAS u2v*)(base + L_PHI + 8192 + o) = u2v{pk2v(pair(en, 4 * q) * an), pk2v(pair(en, 4 * q + 2) * an)};
      *(LAS u2v*)(base + L_V + o) = u2v{pk2(av[4 * q], av[4 * q + 1]), pk2(av[4 * q + 2], av[4 * q + 3])};
    }
  }
  STAMP(8);
  bar();
  STAMP(9);
  if (w < 2) {
    const int q4 = (lane & 15) >> 2, pp = lane & 3, blk = (lane >> 4) & 1;
    u32 ta[2];
#pragma unroll
    for (int t = 0; t < 2; ++t) {
      const int row = 8 * hh + 4 * t + q4;
      ta[t] = row * 64 + (((2 * blk + (pp >> 1)) ^ ((row >> 1) & 3)) << 4) + 8 * (pp & 1);
    }
    f16v kv;
#pragma unroll
    for (int r = 0; r < 16; ++r) kv[r] = 0.f;
    LAS const char* pa = lds + L_PHI + w * 8192;
    LAS const char* pb = lds + L_V;
#pragma unroll
    for (int ks = 0; ks < 8; ++ks)
      kv = mfma32(lds_tr2(pa + ta[0] + ks * 1024, pa + ta[1] + ks * 1024), lds_tr2(pb + ta[0] + ks * 1024, pb + ta[1] + ks * 1024), kv);
    const u32 o = OFF_KVP + ((j * 2 + w) * 16 + i) * 4096;
#pragma unroll
    for (int q = 0; q < 4; ++q)
      st128(rs, vl, o + q * 1024, u4v{__float_as_uint(kv[4 * q]), __float_as_uint(kv[4 * q + 1]), __float_as_uint(kv[4 * q + 2]), __float_as_uint(kv[4 * q + 3])});
  }
  STAMP(18);
  f16v aq;
#pragma unroll
  for (int r = 0; r < 16; ++r) aq[r] = 0.f;
  {
    LAS const char* wb = lds + L_Q + vl;
    bf8 fq[6];
#pragma unroll
    for (int ks = 0; ks < 5; ++ks) fq[ks] = lds_frag(wb + ks * 1024);
#pragma unroll
    for (int ks = 0; ks < 29; ++ks) {
      if (ks + 5 < 28) fq[(ks + 5) % 6] = lds_frag(wb + (ks + 5) * 1024);
      else if (ks + 5 == 28) fq[(ks + 5) % 6] = lds_frag(lds + L_AUG + 6 * 1024 + vl);
      aq = mfma32(fq[ks % 6], ks < 28 ? x[(ks < 28 ? ks : 0) >> 2][ks & 3] : xaug, aq);
      SCHED();
    }
  }
  if (w < 2) flag_set(p.ws, FL_KV + ((j * 2 + w) * 16 + i) * FLS, tag, lane);
  bar();
  STAMP(10);
  bf8 eq[2][2];
  {
    f16v ep, en;
    exp_pair(aq, cexp, ep, en);
    eq[0][0] = frag8(ep, 0); eq[0][1] = frag8(ep, 1);
    eq[1][0] = frag8(en, 0); eq[1][1] = frag8(en, 1);
  }
  STAMP(11);
  const int m64 = R.m64, cc = R.cc;
  const int bm = w & 1, bn = w >> 1;
  u32 ha, wb;
  {
    const int q4 = (lane & 15) >> 2, pp = lane & 3, blk = (lane >> 4) & 1;
    ha = bm * 8192 + bn * 4096 + vl;
    wb = bn * 8192 + (4 * hh + q4) * 128 + (4 * (q4 >> 1) + 2 * blk + (pp >> 1)) * 16 + 8 * (pp & 1);
  }
  const u4v sg2 = ld128(rs, (64 * m64 + 32 * bm + l31) * 16, OFF_ST);
  const u32 b2 = *((const GAS u16*)p.b2 + cc * 64 + bn * 32 + l31);
  f16v g0, g1, a0, a1;
#pragma unroll
  for (int r = 0; r < 16; ++r) { a0[r] = 0.f; a1[r] = 0.f; }
  bf8 fh[4], f0[4], f1[4];
  u32 hp = 0, wp = 0;
  auto point = [&](int g) { hp = hslot(g) + ha; wp = wslot(g) + wb; };
  auto req = [&](int ks) {
    fh[ks] = lds_frag(lds + hp + ks * 1024);
    f0[ks] = lds_tr2(lds + wp + ks * 2048, lds + wp + ks * 2048 + 1024);
    f1[ks] = lds_tr2(lds + (wp ^ 64) + ks * 2048, lds + (wp ^ 64) + ks * 2048 + 1024);
  };
  auto mm = [&](int ks) {
    a0 = mfma32(f0[ks], fh[ks], a0);
    a1 = mfma32(f1[ks], fh[ks], a1);
    __builtin_amdgcn_sched_group_barrier(0x8, 1, 0);
    __builtin_amdgcn_sched_group_barrier(0x100, 3, 0);
    __builtin_amdgcn_sched_group_barrier(0x8, 1, 0);
    __builtin_amdgcn_sched_group_barrier(0x100, 2, 0);
    SCHED();
  };
  auto tick = [&](int g) {
    req(3); mm(0);
    bar_only();
    point(g + 1);
    req(0); mm(1);
    req(1); mm(2);
    req(2); mm(3);
  };
  const u32 krflag = FL_KR + (j * 8 + min(lane, 7)) * FLS;
  int g = 0;
  bool ready = false;
  if (R.bwork) {
    bar_only();
    point(0);
    req(0); req(1); req(2);
    SCHED();
    u64 kr = 0;
#pragma unroll 1
    while (g < 12) {
      tick(g);
      ++g;
      if (__all(kr == tag)) { ready = true; break; }
      if (g >= 4) kr = __hip_atomic_load((GAS u64*)(p.ws + krflag), __ATOMIC_RELAXED, __HIP_MEMORY_SCOPE_AGENT);
    }
  } else {
    const int hs = R.rho;
    flag_wait(p.ws, FL_KV + (hs * 16 + lane) * FLS, 16, tag, lane, 5);
    STAMP(12);
    const u32 o = OFF_KVP + hs * 16 * 4096 + w * 1024;
    u4v t[16];
#pragma unroll
    for (int q = 0; q < 16; ++q) t[q] = ld128(rs, vl, o + q * 4096);
    f2v s0 = {0.f, 0.f}, s1 = {0.f, 0.f};
#pragma unroll
    for (int q = 0; q < 16; ++q) {
      s0 += f2v{__uint_as_float(t[q][0]), __uint_as_float(t[q][1])};
      s1 += f2v{__uint_as_float(t[q][2]), __uint_as_float(t[q][3])};
    }
    const bool zero = (w == 3 && hh == 1) || l31 >= 30, unit = l31 == 29;
    const u32 lo = zero ? 0u : (unit ? 0x3f803f80u : pk2v(s0)), hi = zero ? 0u : (unit ? 0x3f803f80u : pk2v(s1));
    st64(rs, vl, OFF_KVT + (hs * 2 + (w >> 1)) * 1024 + 8 * (w & 1), u2v{lo, hi});
    flag_set(p.ws, FL_KR + (hs * 4 + w) * FLS, tag, lane);
    STAMP(13);
  }
  {
    if (!ready) flag_wait(p.ws, krflag, 8, tag, lane, 6);
    STAMP(14);
    f16v np, nn;
#pragma unroll
    for (int r = 0; r < 16; ++r) { np[r] = 0.f; nn[r] = 0.f; }
    u4v kt[4];
#pragma unroll
    for (int q = 0; q < 4; ++q) kt[q] = ld128(rs, vl, OFF_KVT + (j * 4 + q) * 1024);
    np = mfma32(as_bf8(kt[0]), eq[0][0], np);
    nn = mfma32(as_bf8(kt[2]), eq[1][0], nn);
    np = mfma32(as_bf8(kt[1]), eq[0][1], np);
    nn = mfma32(as_bf8(kt[3]), eq[1][1], nn);
    const float ia = __builtin_amdgcn_rcpf(xhalf_hi(np[13])), ib = __builtin_amdgcn_rcpf(xhalf_hi(nn[13]));
    const float den = xhalf_hi(np[12]) * ia + xhalf_hi(nn[12]) * ib + 1e-6f;
    const float id = __builtin_amdgcn_rcpf(den), fa = ia * id, fb = ib * id;
    u32 ov[8];
#pragma unroll
    for (int r = 0; r < 16; r += 2) ov[r >> 1] = pk2v(pair(np, r) * fa + pair(nn, r) * fb);
    if (hh) { ov[6] = 0u; ov[7] = 0u; }
    const u32 ob = OFF_O + ((4 * i + w) * 32 + 2 * j) * 1024;
    st128(rs, vl, ob, u4v{ov[0], ov[1], ov[2], ov[3]});
    st128(rs, vl, ob + 1024, u4v{ov[4], ov[5], ov[6], ov[7]});
    flag_set(p.ws, FL_O + ((i * 16 + j) * 4 + w) * FLS, tag, lane);
    STAMP(15);
    if (!R.bwork) return;
  }
#pragma unroll 1
  for (; g < 14; ++g) tick(g);
  STAMP(24);
  g0 = a0; g1 = a1;
#pragma unroll
  for (int r = 0; r < 16; ++r) { a0[r] = 0.f; a1[r] = 0.f; }
#pragma unroll 1
  for (; g < 17; ++g) tick(g);
  req(3); mm(0); mm(1); mm(2); mm(3);
  f16v ag, accb;
  {
    LAS char* dst = lds + (bm * 2 + (1 - bn)) * 8192 + vl;
    LAS const char* src = lds + (bm * 2 + bn) * 8192 + vl;
#pragma unroll
    for (int q = 0; q < 4; ++q) {
      const f16v& gs = bn ? g0 : g1;
      const f16v& as = bn ? a0 : a1;
      *(LAS f4v*)(dst + q * 1024) = f4v{gs[4 * q], gs[4 * q + 1], gs[4 * q + 2], gs[4 * q + 3]};
      *(LAS f4v*)(dst + 4096 + q * 1024) = f4v{as[4 * q], as[4 * q + 1], as[4 * q + 2], as[4 * q + 3]};
    }
    bar();
#pragma unroll
    for (int q = 0; q < 4; ++q) {
      const f4v gv = *(LAS const f4v*)(src + q * 1024), sv = *(LAS const f4v*)(src + 4096 + q * 1024);
#pragma unroll
      for (int e = 0; e < 4; ++e) {
        ag[4 * q + e] = (bn ? g1[4 * q + e] : g0[4 * q + e]) + gv[e];
        accb[4 * q + e] = (bn ? a1[4 * q + e] : a0[4 * q + e]) + sv[e];
      }
    }
  }
  STAMP(16);
  {
    float m2 = __uint_as_float(sg2[0]), s2 = __uint_as_float(sg2[1]);
    if (!__all(sg2[2] == (u32)tag && sg2[3] == (u32)(tag >> 32))) stat_wait(p.ws, rs, 64 * m64 + 32 * bm + l31, tag, lane, m2, s2);
    const float var = s2 * s2;
    u32 vh, vlo;
    split2(var, vh, vlo);
    u4v xa2 = {vh | (vlo << 16), 0u, 0u, 0u};
    u4v wa3 = {b2 | (b2 << 16), 0u, 0u, 0u};
    if (hh) { xa2[0] = 0u; wa3[0] = 0u; }
    ag = mfma32(as_bf8(wa3), as_bf8(xa2), ag);
    const float r2 = __builtin_amdgcn_rcpf(var);
    const int row = 32 * bm + l31;
    LAS char* base = lds + L_V + row * 128 + 8 * hh;
    const int key = (row >> 1) & 7;
#pragma unroll
    for (int q = 0; q < 4; ++q) {
      LAS u2v* ptr = (LAS u2v*)(base + (((4 * bn + q) ^ key) << 4));
      const u2v xv = *ptr;
      float o[4];
#pragma unroll
      for (int e = 0; e < 4; ++e) {
        const float gp = ag[4 * q + e] * r2;
        const float sg = gp * __builtin_amdgcn_rcpf(1.f + __builtin_amdgcn_exp2f(-gp * LOG2E));
        const float xr = (e & 1) ? bf_hi(xv[e >> 1]) : bf_lo(xv[e >> 1]);
        o[e] = __builtin_fmaf(accb[4 * q + e], sg, xr);
      }
      *ptr = u2v{pk2(o[0], o[1]), pk2(o[2], o[3])};
    }
  }
  bar();
  {
    const int row = 8 * wave + (lane >> 3);
    u4v v = *(LAS const u4v*)(lds + L_V + row * 128 + (((lane & 7) ^ ((row >> 1) & 7)) << 4));
    *(GAS u4v*)((char*)p.out + (size_t)(64 * m64 + row) * XROW + cc * 128 + (lane & 7) * 16) = v;
  }
  STAMP(17);
  STAMPC(63);
}

__global__ __launch_bounds__(512, 2) void fused_kernel(Params p) {
  extern __shared__ __attribute__((aligned(16))) char smem[];
  LAS char* lds = (LAS char*)smem;
  const int lane = threadIdx.x & 63;
  const int wave = __builtin_amdgcn_readfirstlane(threadIdx.x >> 6);
  const int b = blockIdx.x, chi = b & 7, sidx = b >> 3;
  Role R;
  R.i = 8 * (chi & 1) + (sidx & 7);
  R.j = 4 * (chi >> 1) + (sidx >> 3);
  R.jw = min(R.j, 13);
  R.bwork = sidx < 28;
  R.tq = R.bwork ? sidx / 7 : 0;
  R.cc = R.bwork ? sidx % 7 : 0;
  R.m64 = 4 * chi + R.tq;
  R.rho = 4 * chi + (sidx - 28);
  R.sidx = sidx;
  DIAG_INIT();
  STAMP(0);
  STAMPC(62);
  if (wave >= 4) loader_main(p, (u32)(size_t)lds, R, wave - 4, lane, wave);
  else compute_main(p, lds, R, wave, lane, wave);
  DIAG_DUMP();
}

extern "C" void kernel_launch(void* const* d_in, const int* in_sizes, int n_in, void* d_out, int out_size, void* d_ws,
                              size_t ws_size, hipStream_t stream) {
  Params p;
  p.x = (const u16*)d_in[0];
  p.wqkv = (const u16*)d_in[1];
  p.wphi = (const u16*)d_in[2];
  p.wo = (const u16*)d_in[3];
  p.w1 = (const u16*)d_in[5];
  p.b1 = (const u16*)d_in[6];
  p.w2 = (const u16*)d_in[7];
  p.b2 = (const u16*)d_in[8];
  p.out = (u16*)d_out;
  p.ws = (char*)d_ws;
  (void)hipFuncSetAttribute((const void*)fused_kernel, hipFuncAttributeMaxDynamicSharedMemorySize, LDS_BYTES);
  fused_kernel<<<256, 512, LDS_BYTES, stream>>>(p);
}
```

```cpp
#define TOUCH_POS 1
#include <hip/hip_runtime.h>
#include <hip/hip_bf16.h>
#include <stdint.h>

typedef unsigned short u16;
typedef uint32_t u32;
typedef uint64_t u64;
typedef __bf16 bf2 __attribute__((ext_vector_type(2)));
typedef __bf16 bf4 __attribute__((ext_vector_type(4)));
typedef __bf16 bf8 __attribute__((ext_vector_type(8)));
typedef float f4v __attribute__((ext_vector_type(4)));
typedef float f16v __attribute__((ext_vector_type(16)));
typedef u32 u2v __attribute__((ext_vector_type(2)));
typedef u32 u4v __attribute__((ext_vector_type(4)));
#define LAS __attribute__((address_space(3)))
#define GAS __attribute__((address_space(1)))
#define CAS __attribute__((address_space(4)))
#define DI __device__ __forceinline__

constexpr int S_ = 2048, D_ = 448, NH = 16, DH = 28, FF = 1792;
constexpr int XROW = D_ * 2;
constexpr int QROW = 3 * D_ * 2;
constexpr int W1ROW = FF * 2;

constexpr u32 WS_EPOCH = 0, WS_ERR = 128;
constexpr u32 FL0 = 4096, FLS = 128;
constexpr u32 FL_P0 = FL0;
constexpr u32 FL_C1 = FL_P0 + 768 * FLS;
constexpr u32 FL_H = FL_C1 + 1024 * FLS;
constexpr u32 FL_KV = FL_H + 1024 * FLS;
constexpr u32 FL_KR = FL_KV + 512 * FLS;
constexpr u32 FL_O = FL_KR + 128 * FLS;
constexpr u32 OFF_ST = FL_O + 1024 * FLS;
constexpr u32 OFF_WP = OFF_ST + S_ * 16;
constexpr u32 WP_HEAD = 84 * 1024, WP_Q = 56 * 1024;
constexpr u32 OFF_CSP = OFF_WP + NH * WP_HEAD;
constexpr u32 OFF_C1P = OFF_CSP + NH * 3 * 2048;
constexpr u32 H_TILE = 112 * 1024 + 128, KVP_HS = 16 * 2048 + 128, O_TILE = 32 * 1024 + 128;
constexpr u32 OFF_H = OFF_C1P + 256 * 512;
constexpr u32 OFF_KVP = OFF_H + 64 * H_TILE;
constexpr u32 OFF_KVT = OFF_KVP + NH * 2 * KVP_HS;
constexpr u32 OFF_O = OFF_KVT + NH * 4 * 1024;
constexpr u32 OFF_DIAG = OFF_O + 64 * O_TILE;
constexpr u32 WS_USED = OFF_DIAG + 256 * 8 * 64 * 8;

constexpr u32 SLOT = 16384;
constexpr u32 L_PHI = 8 * SLOT;
constexpr u32 L_V = L_PHI + 16384;
constexpr u32 L_AUG = L_V + 8192;
constexpr u32 L_DUMMY = L_AUG + 7168 + 512;
constexpr u32 L_CNT = L_DUMMY + 256;
constexpr u32 LDS_BYTES = 163840;

constexpr float LOG2E = 1.4426950408889634f;
constexpr u32 SPIN_CAP = 1u << 18;

struct Params {
  const u16 *x, *wqkv, *wphi, *wo, *w1, *b1, *w2, *b2;
  u16* out;
  char* ws;
};

#define STAMP(id)
#define STAMPC(id)
#define DIAG_WAIT()
#define DIAG_INIT()
#define DIAG_DUMP()

DI float bf_lo(u32 u) { return __uint_as_float(u << 16); }
DI float bf_hi(u32 u) { return __uint_as_float(u & 0xffff0000u); }
DI u32 pk2(float a, float b) {
  bf2 v = {(__bf16)a, (__bf16)b};
  return __builtin_bit_cast(u32, v);
}
DI u32 bf_rn(float a) { return pk2(a, 0.f) & 0xffffu; }
DI bf8 as_bf8(u4v v) { return __builtin_bit_cast(bf8, v); }
DI bf8 frag8(const f16v& a, int s) {
  u4v r = {pk2(a[8 * s], a[8 * s + 1]), pk2(a[8 * s + 2], a[8 * s + 3]), pk2(a[8 * s + 4], a[8 * s + 5]), pk2(a[8 * s + 6], a[8 * s + 7])};
  return as_bf8(r);
}
DI f16v mfma32(bf8 a, bf8 b, f16v c) { return __builtin_amdgcn_mfma_f32_32x32x16_bf16(a, b, c, 0, 0, 0); }
DI f4v mfma16(bf8 a, bf8 b, f4v c) { return __builtin_amdgcn_mfma_f32_16x16x32_bf16(a, b, c, 0, 0, 0); }
DI bf8 lds_frag(LAS const char* p) { return *(LAS const bf8*)p; }
DI bf8 lds_tr2(LAS const char* p0, LAS const char* p1) {
  bf4 a = __builtin_amdgcn_ds_read_tr16_b64_v4bf16((LAS bf4*)p0);
  bf4 b = __builtin_amdgcn_ds_read_tr16_b64_v4bf16((LAS bf4*)p1);
  return __builtin_shufflevector(a, b, 0, 1, 2, 3, 4, 5, 6, 7);
}
DI float xhalf_max(float v) {
  auto r = __builtin_amdgcn_permlane32_swap(__float_as_uint(v), __float_as_uint(v), false, false);
  return __builtin_elementwise_maximum(__uint_as_float(r[0]), __uint_as_float(r[1]));
}
DI float xhalf_min(float v) {
  auto r = __builtin_amdgcn_permlane32_swap(__float_as_uint(v), __float_as_uint(v), false, false);
  return __builtin_elementwise_minimum(__uint_as_float(r[0]), __uint_as_float(r[1]));
}
DI float xhalf_sum(float v) {
  auto r = __builtin_amdgcn_permlane32_swap(__float_as_uint(v), __float_as_uint(v), false, false);
  return __uint_as_float(r[0]) + __uint_as_float(r[1]);
}
DI float xhalf_hi(float v) {
  auto r = __builtin_amdgcn_permlane32_swap(__float_as_uint(v), __float_as_uint(v), false, false);
  return __uint_as_float(r[1]);
}
DI float dpp_ror(float v, int n) {
  const int iv = __builtin_bit_cast(int, v);
  int r;
  switch (n) {
    case 8: r = __builtin_amdgcn_update_dpp(0, iv, 0x128, 0xf, 0xf, false); break;
    case 4: r = __builtin_amdgcn_update_dpp(0, iv, 0x124, 0xf, 0xf, false); break;
    case 2: r = __builtin_amdgcn_update_dpp(0, iv, 0x122, 0xf, 0xf, false); break;
    default: r = __builtin_amdgcn_update_dpp(0, iv, 0x121, 0xf, 0xf, false); break;
  }
  return __builtin_bit_cast(float, r);
}
DI float half_sum(float v) {
  v += dpp_ror(v, 8); v += dpp_ror(v, 4); v += dpp_ror(v, 2); v += dpp_ror(v, 1);
  auto r = __builtin_amdgcn_permlane16_swap(__float_as_uint(v), __float_as_uint(v), false, false);
  return __uint_as_float(r[0]) + __uint_as_float(r[1]);
}
typedef float f2v __attribute__((ext_vector_type(2)));
DI f2v pair(const f16v& a, int r) { return f2v{a[r], a[r + 1]}; }
DI u32 pk2v(f2v v) { return pk2(v[0], v[1]); }
DI void bar() { asm volatile("s_waitcnt lgkmcnt(0)\n\ts_barrier" ::: "memory"); }
DI void bar_only() { asm volatile("s_barrier" ::: "memory"); }
template <int N> DI void vmwait() { asm volatile("s_waitcnt vmcnt(%0)" ::"i"(N) : "memory"); }
#define SCHED() __builtin_amdgcn_sched_barrier(0)

DI void dma(u32 voff, const void* sbase, u32 ldsaddr) {
  asm volatile("s_mov_b32 m0, %2\n\ts_nop 0\n\tglobal_load_lds_dwordx4 %0, %1" ::"v"(voff), "s"(sbase), "s"(ldsaddr) : "memory");
}
DI void dma_sc1(u32 voff, const void* sbase, u32 ldsaddr) {
  asm volatile("s_mov_b32 m0, %2\n\ts_nop 0\n\tglobal_load_lds_dwordx4 %0, %1 sc1" ::"v"(voff), "s"(sbase), "s"(ldsaddr) : "memory");
}

DI void dma4(u32 v0, u32 v1, u32 v2, u32 v3, const void* sbase, u32 ldsaddr) {
  asm volatile("s_mov_b32 m0, %5\n\ts_nop 0\n\tglobal_load_lds_dwordx4 %0, %4\n\tglobal_load_lds_dwordx4 %1, %4 offset:1024\n\t"
               "global_load_lds_dwordx4 %2, %4 offset:2048\n\tglobal_load_lds_dwordx4 %3, %4 offset:3072"
               ::"v"(v0), "v"(v1), "v"(v2), "v"(v3), "s"(sbase), "s"(ldsaddr) : "memory");
}
template <int N> DI void dma_lin(u32 v, const void* sbase, u32 ldsaddr) {
  if constexpr (N == 1)
    asm volatile("s_mov_b32 m0, %2\n\ts_nop 0\n\tglobal_load_lds_dwordx4 %0, %1 sc1" ::"v"(v), "s"(sbase), "s"(ldsaddr) : "memory");
  else if constexpr (N == 2)
    asm volatile("s_mov_b32 m0, %2\n\ts_nop 0\n\tglobal_load_lds_dwordx4 %0, %1 sc1\n\tglobal_load_lds_dwordx4 %0, %1 offset:1024 sc1"
                 ::"v"(v), "s"(sbase), "s"(ldsaddr) : "memory");
  else
    asm volatile("s_mov_b32 m0, %2\n\ts_nop 0\n\tglobal_load_lds_dwordx4 %0, %1 sc1\n\tglobal_load_lds_dwordx4 %0, %1 offset:1024 sc1\n\t"
                 "global_load_lds_dwordx4 %0, %1 offset:2048 sc1\n\tglobal_load_lds_dwordx4 %0, %1 offset:3072 sc1"
                 ::"v"(v), "s"(sbase), "s"(ldsaddr) : "memory");
}

typedef __amdgpu_buffer_rsrc_t rsrc_t;
DI u4v ld128(rsrc_t r, u32 voff, u32 soff) { return __builtin_amdgcn_raw_buffer_load_b128(r, voff, soff, 16); }
DI u32 ld32(rsrc_t r, u32 voff, u32 soff) { return __builtin_amdgcn_raw_buffer_load_b32(r, voff, soff, 16); }
DI void st128(rsrc_t r, u32 voff, u32 soff, u4v v) { __builtin_amdgcn_raw_buffer_store_b128(v, r, voff, soff, 16); }
DI void st64(rsrc_t r, u32 voff, u32 soff, u2v v) { __builtin_amdgcn_raw_buffer_store_b64(v, r, voff, soff, 16); }
DI void st32(rsrc_t r, u32 voff, u32 soff, u32 v) { __builtin_amdgcn_raw_buffer_store_b32(v, r, voff, soff, 16); }
DI float fmx(float a, float b) { return __builtin_elementwise_maximum(a, b); }
DI float fmn(float a, float b) { return __builtin_elementwise_minimum(a, b); }
DI void touch(u32 voff, const void* sbase, u32 lb) {
  asm volatile("s_mov_b32 m0, %2\n\ts_nop 0\n\tglobal_load_lds_dword %0, %1" ::"v"(voff), "s"(sbase), "s"(lb + L_DUMMY) : "memory");
}

DI void cnt_arrive(u32 addr, int lane) {
  asm volatile("s_waitcnt lgkmcnt(0)" ::: "memory");
  if (lane == 0) __hip_atomic_fetch_add((LAS u32*)(size_t)addr, 1u, __ATOMIC_RELAXED, __HIP_MEMORY_SCOPE_WORKGROUP);
}
DI void cnt_wait(u32 addr) {
#pragma unroll 1
  for (u32 spin = 0; spin < SPIN_CAP; ++spin)
    if (__builtin_amdgcn_readfirstlane(__hip_atomic_load((LAS u32*)(size_t)addr, __ATOMIC_RELAXED, __HIP_MEMORY_SCOPE_WORKGROUP)) >= 4u) break;
  asm volatile("" ::: "memory");
}
DI u64 tag64(u32 t) { return (u64)t | ((u64)(t * 0x9E3779B1u + 0x7F4A7C15u) << 32); }

DI void flag_set(char* ws, u32 off, u64 tag, int lane) {
  asm volatile("s_waitcnt vmcnt(0)" ::: "memory");
  if (lane == 0) __hip_atomic_store((GAS u64*)(ws + off), tag, __ATOMIC_RELAXED, __HIP_MEMORY_SCOPE_AGENT);
}
DI void flag_wait(char* ws, u32 off, int n, u64 tag, int lane, u32 code) {
  GAS u64* ptr = (GAS u64*)(ws + off);
#pragma unroll 1
  for (u32 spin = 0;; ++spin) {
    u64 v = tag;
    if (lane < n) v = __hip_atomic_load(ptr, __ATOMIC_RELAXED, __HIP_MEMORY_SCOPE_AGENT);
    if (__all(v == tag)) break;
    if (spin > SPIN_CAP) {
      if (lane == 0) __hip_atomic_store((GAS u32*)(ws + WS_ERR), code, __ATOMIC_RELAXED, __HIP_MEMORY_SCOPE_AGENT);
      break;
    }
    __builtin_amdgcn_s_sleep(1);
  }
  asm volatile("" ::: "memory");
}
DI void stat_wait(char* ws, rsrc_t rs, int token, u64 tag, int lane, float& mean, float& sigma) {
#pragma unroll 1
  for (u32 spin = 0;; ++spin) {
    u4v g = ld128(rs, token * 16, OFF_ST);
    mean = __uint_as_float(g[0]);
    sigma = __uint_as_float(g[1]);
    bool ok = g[2] == (u32)tag && g[3] == (u32)(tag >> 32);
    if (__all(ok)) break;
    if (spin > SPIN_CAP) {
      if (lane == 0) __hip_atomic_store((GAS u32*)(ws + WS_ERR), 99u, __ATOMIC_RELAXED, __HIP_MEMORY_SCOPE_AGENT);
      break;
    }
    __builtin_amdgcn_s_sleep(1);
  }
}
DI void split2(float v, u32& hi, u32& lo) {
  hi = bf_rn(v);
  lo = bf_rn(v - __uint_as_float(hi << 16));
}
DI bf8 aug_w(float cs, u32 beta, int hh) {
  u32 hi, lo;
  split2(cs, hi, lo);
  u4v r = {hi | (hi << 16), lo | (beta << 16), beta, 0u};
  if (hh) r = u4v{0u, 0u, 0u, 0u};
  return as_bf8(r);
}

struct Role {
  int i, j;
  int jw;
  int m64, cc;
  int tq;
  bool bwork;
  int rho;
  int sidx;
};

DI u32 wslot(int g) { return (u32)((0x1076u >> (4 * (g & 3))) & 15) * SLOT; }
DI u32 hslot(int g) { return (u32)((0x3254u >> (4 * (g & 3))) & 15) * SLOT; }
DI constexpr u32 fslot(int c) { return (u32)(2 * (c % 4)) * SLOT; }
constexpr u32 L_KVA = 6 * SLOT;
constexpr u32 L_KVB = 0;
constexpr u32 L_Q = 2 * SLOT;
constexpr u32 L_HS = 4 * SLOT;

DI void loader_main(const Params& p, u32 lb, const Role& R, int lw, int lane, int wave) {
  const int i = R.i, j = R.j, m64 = R.m64, cc = R.cc;
  const u32 vx0 = (lane >> 3) * XROW + (((lane & 7) ^ (lane >> 4)) << 4);
  const u32 vx1 = (vx0 ^ 64) + (8 * XROW - 1024), vx2 = vx0 + 2 * (8 * XROW - 1024), vx3 = vx1 + 2 * (8 * XROW - 1024);
  const u32 vw0 = (lane >> 4) * W1ROW + (((lane & 15) ^ ((lane >> 4) << 2)) << 4);
  const u32 vw1 = vw0 + (4 * W1ROW - 1024), vw2 = vw0 + 2 * (4 * W1ROW - 1024), vw3 = vw0 + 3 * (4 * W1ROW - 1024);
  const char* xb = (const char*)p.x + (size_t)(128 * i + 32 * lw) * XROW;
  const char* w1b = (const char*)p.w1 + (size_t)(16 * lw) * W1ROW + R.jw * 256;
  const u32 lq = lb + lw * 4096;
  int gc = 0;
  auto stage = [&](u32 slot) {
    dma4(vx0, vx1, vx2, vx3, xb + gc * 128, lq + slot);
    dma4(vw0, vw1, vw2, vw3, w1b + (size_t)gc * (64 * W1ROW), lq + slot + SLOT);
    ++gc;
  };
  if (lw == 3) {
    const int t = R.sidx * 6 + min(lane, 5);
    const char* q;
    if (t < 28) q = (const char*)p.x + t * 65536;
    else if (t < 47) q = (const char*)p.wqkv + (t - 28) * 63000;
    else if (t < 48) q = (const char*)p.wphi;
    else if (t < 54) q = (const char*)p.wo + (t - 48) * 65536;
    else if (t < 79) q = (const char*)p.w1 + (t - 54) * 64000;
    else if (t < 80) q = (const char*)p.b1;
    else if (t < 105) q = (const char*)p.w2 + (t - 80) * 64000;
    else if (t < 106) q = (const char*)p.b2;
    else if (t < 134) q = (const char*)p.out + (t - 106) * 65536;
    else if (t < 166) q = p.ws + OFF_H + (size_t)(2 * (t - 134) + 1) * H_TILE - 128;
    else if (t < 174) q = p.ws + OFF_KVP + (size_t)(4 * (t - 166) + 1) * KVP_HS - 128;
    else if (t < 182) q = p.ws + OFF_O + (size_t)(8 * (t - 174) + 1) * O_TILE - 128;
    else q = p.ws + 512;
    asm volatile("s_mov_b32 m0, %1\n\ts_nop 0\n\tglobal_load_lds_dword %0, off" ::"v"(q), "s"(lb + L_DUMMY) : "memory");
  }
  auto warm = [&](int c0) {
    const int lx = min(lane, 8 * (7 - c0) - 1), lz = min(lane, 4 * (7 - c0) - 1);
    touch((32 * (j & 3) + 8 * lw + (lx & 7)) * XROW + (c0 + (lx >> 3)) * 128, (const char*)p.x + (size_t)(128 * i) * XROW, lb);
    touch((64 * (c0 + (lz >> 2)) + 8 * (i & 7) + 2 * lw + ((lz & 3) >> 1)) * W1ROW + (lz & 1) * 128, (const char*)p.w1 + R.jw * 256, lb);
  };
  bar_only();
  STAMP(12);
  stage(fslot(0));
  STAMP(13);
  warm(1);
  stage(fslot(1));
  const u32 E = *(CAS const u32*)(p.ws + WS_EPOCH);
  const u64 tag = tag64(E + 1);
  const rsrc_t rs = __builtin_amdgcn_make_buffer_rsrc(p.ws, 0, WS_USED, 0x00020000);
  const u32 vl = lane * 16;
  const u32 bchunk = ((lane & 7) ^ (((lane >> 4) & 1) << 2)) << 4;
  const u32 vb0 = (lane >> 3) * XROW + bchunk;
  const u32 vb1 = vb0 + (8 * XROW - 1024), vb2 = vb0 + 2 * (8 * XROW - 1024), vb3 = vb0 + 3 * (8 * XROW - 1024);
  const u32 vb3c = min(lane >> 3, 3) * XROW + bchunk + 3 * (8 * XROW - 1024);
  STAMP(1);
  vmwait<10>(); bar_only();
  STAMP(2);
  stage(fslot(2)); vmwait<8>(); bar_only();
  stage(fslot(3)); vmwait<8>(); bar_only();
  stage(fslot(4)); vmwait<8>(); bar_only();
  stage(fslot(5)); vmwait<8>(); bar_only();
  stage(fslot(6));
  const char* wp = p.ws + OFF_WP + (size_t)j * WP_HEAD;
  const u32 pflag = lane < 42 ? FL_P0 + (j * 48 + (lane / 14) * 16 + lane % 14) * FLS : FL_C1 + (R.jw * 16 + min(lane - 42, 15)) * FLS;
  dma_sc1(pflag, p.ws, lb + L_V + 4096 + lw * 1024);
  vmwait<9>(); bar_only();
  STAMP(3);
  vmwait<0>();
  {
    const u2v f = *(LAS const u2v*)(size_t)(lb + L_V + 4096 + lw * 1024 + lane * 16);
    if (!__all(f[0] == (u32)tag && f[1] == (u32)(tag >> 32))) flag_wait(p.ws, pflag, 58, tag, lane, 1);
  }
  STAMP(4);
  bar_only();
  const u32 gs = lb + L_PHI + lw * 4096;
  {
    const char* c1 = p.ws + OFF_C1P + (size_t)R.jw * 16 * 512 + lw * 128;
    const u32 vg = (lane >> 3) * 512 + (lane & 7) * 16;
    dma_lin<1>(vg, c1, gs);
    dma_lin<1>(vg, c1 + 8 * 512, gs + 1024);
    dma_lin<2>(vl, p.ws + OFF_CSP + (size_t)(j * 3 + min(lw, 2)) * 2048, gs + 2048);
  }
  asm volatile("s_mov_b32 m0, %2\n\ts_nop 0\n\tglobal_load_lds_dword %0, %1" ::"v"(min(lane, 15) * 4), "s"((const char*)p.b1 + (R.jw * 128 + lw * 32) * 2),
               "s"(lb + L_V + lw * 256) : "memory");
#pragma unroll
  for (int c = 0; c < 2; ++c) dma_lin<2>(vl, wp + c * 8192 + lw * 2048, lb + L_KVA + c * 8192 + lw * 2048);
  vmwait<4>();
  {
    const int hh = lane >> 5, l31 = lane & 31;
    LAS const float* src = (LAS const float*)(size_t)(gs + hh * 1024 + l31 * 4);
    float a = 0.f, b = 0.f;
#pragma unroll
    for (int q = 0; q < 8; ++q) a += src[32 * q];
    LAS const float* src2 = (LAS const float*)(size_t)(gs + 2048 + hh * 7 * 128 + l31 * 4);
#pragma unroll
    for (int q = 0; q < 7; ++q) b += src2[32 * q];
    const u32 beta1 = *(LAS const u16*)(size_t)(lb + L_V + lw * 256 + l31 * 2);
    LAS char* aug = (LAS char*)(size_t)(lb + L_AUG) + vl;
    *(LAS bf8*)(aug + lw * 1024) = aug_w(xhalf_sum(a), beta1, hh);
    if (lw < 3) *(LAS bf8*)(aug + (4 + lw) * 1024) = aug_w(xhalf_sum(b), (lw == 1 && l31 == 28) ? 0x3f80u : 0u, hh);
    asm volatile("s_waitcnt lgkmcnt(0)" ::: "memory");
  }
  bar_only();
  STAMP(5);
#pragma unroll
  for (int c = 2; c < 4; ++c) dma_lin<2>(vl, wp + c * 8192 + lw * 2048, lb + L_KVA + c * 8192 + lw * 2048);
#pragma unroll
  for (int c = 4; c < 7; ++c) dma_lin<2>(vl, wp + c * 8192 + lw * 2048, lb + L_KVB + (c - 4) * 8192 + lw * 2048);
#pragma unroll
  for (int c = 0; c < 7; ++c) dma_lin<1>(vl, wp + WP_Q + c * 4096 + lw * 1024, lb + L_Q + c * 4096 + lw * 1024);
  if (R.bwork) {
#pragma unroll 1
    for (int q = lw; q < 9; q += 4) {
      if (q < 7) touch(lane * XROW, (const char*)p.w2 + (size_t)(448 * R.tq + 64 * q) * XROW + cc * 128, lb);
      else touch(min(lane, q == 7 ? 63 : 47) * XROW, (const char*)p.wo + (size_t)(112 * R.tq + 64 * (q - 7)) * XROW + cc * 128, lb);
    }
    if (lw == 0) vmwait<10>(); else vmwait<9>();
  } else {
    vmwait<7>();
  }
  bar_only();
  if (j < 14) {
    LAS const char* src = (LAS const char*)(size_t)(lb + L_HS + lw * 8192) + vl;
    const u32 hb = OFF_H + (4 * i + lw) * H_TILE + 8 * j * 1024;
    u4v hv[8];
#pragma unroll
    for (int q = 0; q < 8; ++q) hv[q] = *(LAS const u4v*)(src + q * 1024);
#pragma unroll
    for (int q = 0; q < 8; ++q) st128(rs, vl, hb + q * 1024, hv[q]);
  }
  STAMP(14);
  vmwait<0>();
  if (j < 14 && lane == 0) __hip_atomic_store((GAS u64*)(p.ws + FL_H + ((i * 16 + j) * 4 + lw) * FLS), tag, __ATOMIC_RELAXED, __HIP_MEMORY_SCOPE_AGENT);
  cnt_arrive(lb + L_CNT + 8, lane);
  cnt_wait(lb + L_CNT + 4);
  STAMP(6);
  const char* w2p = (const char*)p.w2 + (size_t)(32 * lw) * XROW + cc * 128;
  auto issue_w = [&](int g) {
    if (g < 14) {
      dma4(vb0, vb1, vb2, vb3, w2p, lq + wslot(g));
      w2p += 128 * XROW;
    } else {
      dma4(vb0, vb1, vb2, vb3c, (const char*)p.wo + (size_t)(28 * (4 * (g - 14) + lw)) * XROW + cc * 128, lq + wslot(g));
    }
  };
  const char* hp = p.ws + OFF_H + (size_t)(2 * m64 + (lw >> 1)) * H_TILE + (lw & 1) * 4096;
  auto issue_h = [&](int g) {
    if (g == 14) hp = p.ws + OFF_O + (size_t)(2 * m64 + (lw >> 1)) * O_TILE + (lw & 1) * 4096;
    dma_lin<4>(vl, hp, lq + hslot(g));
    hp += 8192;
  };
  if (!R.bwork) return;
  issue_w(0);
  issue_w(1);
  issue_w(2);
  issue_w(3);
  flag_wait(p.ws, FL_H + (((m64 >> 1) * 16 + (lane >> 1)) * 4 + 2 * (m64 & 1) + (lane & 1)) * FLS, 28, tag, lane, 2);
  STAMP(7);
  cnt_wait(lb + L_CNT + 8);
  issue_h(0);
  touch(lane * 128, p.ws + OFF_H + (size_t)(2 * m64 + (lw >> 1)) * H_TILE + (16 * cc + 8 * (lw & 1)) * 1024, lb);
  issue_h(1);
  vmwait<5>(); bar_only();
  issue_h(2);
  vmwait<4>(); bar_only();
  issue_h(3);
  {
    const char* b = (const char*)p.x + (size_t)(64 * m64 + 16 * lw) * XROW + cc * 128;
    dma(vx0, b, lb + L_V + lw * 2048);
    dma(vx0 ^ 64, b + 8 * XROW, lb + L_V + lw * 2048 + 1024);
  }
  STAMP(8);
  vmwait<6>(); bar_only();
#pragma unroll 1
  for (int g = 2; g < 12; ++g) {
    issue_h(g + 2);
    issue_w(g + 2);
    vmwait<8>();
    bar_only();
  }
  issue_w(14);
  vmwait<4>(); bar_only();
  issue_w(15);
  dma4(vb0, vb1, vb2, vb3c, (const char*)p.wo + (size_t)(28 * (8 + lw)) * XROW + cc * 128, lb + L_PHI + lw * 4096);
  flag_wait(p.ws, FL_O + (((m64 >> 1) * 16 + (lane >> 1)) * 4 + 2 * (m64 & 1) + (lane & 1)) * FLS, 32, tag, lane, 3);
  STAMP(9);
  issue_h(14);
  issue_h(15);
  {
    const int line = min((4 * cc + lw) * 19 + min(lane, 18), 511);
    touch(line * 128 + (line >> 8) * 128, p.ws + OFF_O + (size_t)(2 * m64) * O_TILE, lb);
  }
  vmwait<5>(); bar_only();
  issue_h(16);
  issue_w(17);
  issue_h(17);
  vmwait<12>(); bar_only();
  vmwait<8>(); bar_only();
  vmwait<0>(); bar_only();
  bar_only();
  STAMP(10);
  bar_only();
  {
    LAS const char* lds = (LAS const char*)(size_t)lb;
    const int row = 8 * wave + (lane >> 3);
    u4v v = *(LAS const u4v*)(lds + L_V + row * 128 + (((lane & 7) ^ ((row >> 1) & 7)) << 4));
    *(GAS u4v*)((char*)p.out + (size_t)(64 * m64 + row) * XROW + cc * 128 + (lane & 7) * 16) = v;
  }
  if (blockIdx.x == 0 && lw == 0 && lane == 0)
    __hip_atomic_store((GAS u32*)(p.ws + WS_EPOCH), E + 1, __ATOMIC_RELAXED, __HIP_MEMORY_SCOPE_AGENT);
  STAMP(11);
}

DI void exp_pair(const f16v& a, float c, f16v& ep, f16v& en) {
  float mx = fmx(a[0], a[1]), mn = fmn(a[0], a[1]);
#pragma unroll
  for (int r = 2; r < 16; r += 2) {
    mx = fmx(fmx(mx, a[r]), a[r + 1]);
    mn = fmn(fmn(mn, a[r]), a[r + 1]);
  }
  mx = xhalf_max(mx);
  mn = xhalf_min(mn);
  const f2v c2 = {c, c}, bp = {-mx * c, -mx * c}, bn = {mn * c, mn * c};
#pragma unroll
  for (int r = 0; r < 16; r += 2) {
    const f2v tp = __builtin_elementwise_fma(pair(a, r), c2, bp), tn = __builtin_elementwise_fma(pair(a, r), -c2, bn);
    ep[r] = __builtin_amdgcn_exp2f(tp[0]); ep[r + 1] = __builtin_amdgcn_exp2f(tp[1]);
    en[r] = __builtin_amdgcn_exp2f(tn[0]); en[r + 1] = __builtin_amdgcn_exp2f(tn[1]);
  }
}
DI float sum28(const f16v& e, int hh) {
  const f2v s0 = (pair(e, 0) + pair(e, 2)) + (pair(e, 4) + pair(e, 6)), s1 = pair(e, 8) + pair(e, 10), s2 = pair(e, 12) + pair(e, 14);
  const f2v t = s0 + s1;
  return xhalf_sum((t[0] + t[1]) + (hh ? 0.f : s2[0] + s2[1]));
}

DI void fold_load(const Params& p, u32 lb, int i, int head, int part, int lane, u4v (&a)[2]) {
  const int r16 = lane & 15, dg = lane >> 4;
  const int col = part == 0 ? XROW : (part == 1 ? 2 * XROW : 0);
  const char* src = (const char*)p.wqkv + (size_t)(32 * i + r16) * QROW + col + head * 56 + (dg == 3 ? 40 : dg * 16);
  a[0] = *(GAS const u4v*)src;
  a[1] = *(GAS const u4v*)(src + 16 * QROW);
  const char* wp = (const char*)p.wphi + head * 1568;
  const u32 dst = lb + L_PHI + part * 2048;
  dma(lane * 16, wp, dst);
  dma(min(lane, 33) * 16, wp + 1024, dst + 1024);
}
DI void fold_run(rsrc_t rs, LAS const char* lds, int i, int head, int part, int lane, const u4v (&a)[2]) {
  const int dg = lane >> 4, r16 = lane & 15;
  asm volatile("s_waitcnt vmcnt(0)" ::: "memory");
  u4v bw[2];
  {
    const int q4 = r16 >> 2, pp = lane & 3;
    LAS const char* b = lds + L_PHI + part * 2048 + (8 * dg + q4) * 56 + pp * 8;
#pragma unroll
    for (int ft = 0; ft < 2; ++ft) {
      u4v v = __builtin_bit_cast(u4v, lds_tr2(b + ft * 32, b + ft * 32 + 4 * 56));
      if (dg == 3) { v[2] = 0u; v[3] = 0u; }
      if (part == 1) {
        const int t = 16 * ft + r16 - 8 * dg;
        const u32 one = (16 * ft + r16 < 28) ? ((t & 1) ? 0x3f800000u : 0x3f80u) : 0u;
#pragma unroll
        for (int q = 0; q < 4; ++q) v[q] = (t >> 1) == q ? one : 0u;
      }
      bw[ft] = v;
    }
  }
  const u32 base = OFF_WP + head * WP_HEAD + (part == 2 ? WP_Q + 2 * i * 1024 : 2 * i * 2048 + part * 1024);
  const u32 step = part == 2 ? 1024 : 2048;
  float cs = 0.f;
#pragma unroll
  for (int t = 0; t < 2; ++t) {
    u4v av = a[t];
    if (dg == 3) av = u4v{av[2], av[3], 0u, 0u};
    const f4v z = {0.f, 0.f, 0.f, 0.f};
    f4v d0 = mfma16(as_bf8(av), as_bf8(bw[0]), z), d1 = mfma16(as_bf8(av), as_bf8(bw[1]), z);
    if (part != 1) {
#pragma unroll
      for (int r = 0; r < 4; ++r) {
        const int v = __float_as_int(d1[r]);
        d1[r] = __int_as_float(__builtin_amdgcn_update_dpp(v, v, 0x15B, 0xf, 0x8, false));
      }
    }
    auto s0 = __builtin_amdgcn_permlane16_swap(pk2(d0[0], d0[1]), pk2(d1[0], d1[1]), false, false);
    auto s1 = __builtin_amdgcn_permlane16_swap(pk2(d0[2], d0[3]), pk2(d1[2], d1[3]), false, false);
    const u4v o = {s0[0], s1[0], s0[1], s1[1]};
    st128(rs, lane * 16, base + t * step, o);
    cs += ((bf_lo(o[0]) + bf_hi(o[0])) + (bf_lo(o[1]) + bf_hi(o[1]))) + ((bf_lo(o[2]) + bf_hi(o[2])) + (bf_lo(o[3]) + bf_hi(o[3])));
  }
  cs = xhalf_sum(cs);
  if (lane < 32) st32(rs, lane * 4, OFF_CSP + ((head * 3 + part) * 16 + i) * 128, __float_as_uint(cs));
}

constexpr int FD = 6, FNB = FD + 1;
template <int U>
DI void f_request(LAS const char* lds, u32 (&xa)[4], u32 (&wa)[4], bf8 (&x)[7][4], bf8 (&wf)[FNB]) {
  if constexpr (U < 112) {
    constexpr int c = U >> 4, s = (U >> 2) & 3, n = U & 3;
    if constexpr ((U & 31) == 0 && U > 0) {
#pragma unroll
      for (int q = 0; q < 4; ++q) {
        xa[q] ^= 65536u;
        wa[q] ^= 65536u;
        asm volatile("" : "+v"(xa[q]), "+v"(wa[q]));
      }
    }
    constexpr u32 xs = fslot(c) & 65535u, ws = xs + SLOT;
    if constexpr (n == 0) x[c][s] = lds_frag(lds + xs + xa[s]);
    wf[U % FNB] = lds_tr2(lds + ws + wa[n] + 4096 * s, lds + ws + wa[n] + 4096 * s + 1024);
  }
}
template <int C, int V>
DI void f_unit(LAS const char* lds, u32 (&xa)[4], u32 (&wa)[4], bf8 (&x)[7][4], bf8 (&wf)[FNB], f16v (&acc)[4]) {
  constexpr int U = 16 * C + V;
  if constexpr (V == 16 - FD && C < 6) bar_only();
  f_request<U + FD>(lds, xa, wa, x, wf);
  acc[V & 3] = mfma32(wf[U % FNB], x[C][V >> 2], acc[V & 3]);
  SCHED();
  if constexpr (V < 15) f_unit<C, V + 1>(lds, xa, wa, x, wf, acc);
}
template <int C>
DI void tick_f(LAS const char* lds, u32 (&xa)[4], u32 (&wa)[4], bf8 (&x)[7][4], bf8 (&wf)[FNB], f16v (&acc)[4]) {
  f_unit<C, 0>(lds, xa, wa, x, wf, acc);
}
template <int U>
DI void f_prime(LAS const char* lds, u32 (&xa)[4], u32 (&wa)[4], bf8 (&x)[7][4], bf8 (&wf)[FNB]) {
  f_request<U>(lds, xa, wa, x, wf);
  if constexpr (U + 1 < FD) f_prime<U + 1>(lds, xa, wa, x, wf);
}

DI void compute_main(const Params& p, LAS char* lds, const Role& R, int w, int lane, int wave) {
  const rsrc_t rs = __builtin_amdgcn_make_buffer_rsrc(p.ws, 0, WS_USED, 0x00020000);
  const int i = R.i, j = R.j;
  const int hh = lane >> 5, l31 = lane & 31;
  const bool ffn = j < 14;
  const bool fold = i < 14 && w < 3;
  const u32 E = *(CAS const u32*)(p.ws + WS_EPOCH);
  const u64 tag = tag64(E + 1);
  u32 xs7[7];
  const int stok = 128 * i + 8 * j + 2 * w + hh;
#pragma unroll
  for (int m = 0; m < 7; ++m) xs7[m] = *(GAS const u32*)((const char*)p.x + (size_t)stok * XROW + m * 128 + l31 * 4);
  auto stats = [&]() {
    f2v s2 = {0.f, 0.f};
#pragma unroll
    for (int m = 0; m < 7; ++m) s2 += f2v{bf_lo(xs7[m]), bf_hi(xs7[m])};
    const float mean = half_sum(s2[0] + s2[1]) * (1.f / D_);
    f2v q2 = {0.f, 0.f};
#pragma unroll
    for (int m = 0; m < 7; ++m) {
      const f2v a = f2v{bf_lo(xs7[m]), bf_hi(xs7[m])} - mean;
      q2 = __builtin_elementwise_fma(a, a, q2);
    }
    const float sigma = __builtin_amdgcn_sqrtf(half_sum(q2[0] + q2[1]) * (1.f / D_) + 1e-5f);
    if (l31 == 0) st128(rs, stok * 16, OFF_ST, u4v{__float_as_uint(mean), __float_as_uint(sigma), (u32)tag, (u32)(tag >> 32)});
  };
  if (fold) {
    u4v fa[2];
    fold_load(p, (u32)(size_t)lds, i, j, w, lane, fa);
    SCHED();
    bar_only();
    SCHED();
    STAMP(22);
    fold_run(rs, lds, i, j, w, lane, fa);
    stats();
  } else if (w < 3) {
    bar_only();
    stats();
  } else {
    u4v fc[7];
    const char* src = (const char*)p.w1 + (size_t)(28 * i + (lane >> 4)) * W1ROW + R.jw * 256 + (lane & 15) * 16;
#pragma unroll
    for (int q = 0; q < 7; ++q) fc[q] = *(GAS const u4v*)(src + (size_t)q * 4 * W1ROW);
    SCHED();
    bar_only();
    SCHED();
    float cs[8];
#pragma unroll
    for (int e = 0; e < 8; ++e) cs[e] = 0.f;
#pragma unroll
    for (int q = 0; q < 7; ++q)
#pragma unroll
      for (int e = 0; e < 4; ++e) { cs[2 * e] += bf_lo(fc[q][e]); cs[2 * e + 1] += bf_hi(fc[q][e]); }
#pragma unroll
    for (int e = 0; e < 8; ++e) {
      auto r = __builtin_amdgcn_permlane16_swap(__float_as_uint(cs[e]), __float_as_uint(cs[e]), false, false);
      cs[e] = xhalf_sum(__uint_as_float(r[0]) + __uint_as_float(r[1]));
    }
    if (ffn && lane < 32) {
      const int o = (lane >> 4) * 4;
      st128(rs, (lane & 15) * 32 + (lane >> 4) * 16, OFF_C1P + (j * 16 + i) * 512,
            u4v{__float_as_uint(o ? cs[4] : cs[0]), __float_as_uint(o ? cs[5] : cs[1]), __float_as_uint(o ? cs[6] : cs[2]), __float_as_uint(o ? cs[7] : cs[3])});
    }
    stats();
  }
  STAMP(23);
  u32 xa[4], wa[4];
  {
    const int row = 32 * w + l31;
    const u32 b = row * 128 + ((hh ^ ((row >> 1) & 7)) << 4);
#pragma unroll
    for (int s = 0; s < 4; ++s) xa[s] = b ^ (32 * s);
    const int q4 = (lane & 15) >> 2, pp = lane & 3, blk = (lane >> 4) & 1;
#pragma unroll
    for (int n = 0; n < 4; ++n) {
      const int nt = (n + w) & 3;
      wa[n] = (8 * hh + q4) * 256 + (4 * (nt ^ q4) + 2 * blk + (pp >> 1)) * 16 + 8 * (pp & 1);
    }
  }
  bf8 x[7][4], wf[FNB];
  f16v acc[4];
#pragma unroll
  for (int n = 0; n < 4; ++n)
#pragma unroll
    for (int r = 0; r < 16; ++r) acc[n][r] = 0.f;

  if (w == 0 && lane < 4) *(LAS volatile u32*)(lds + L_CNT + lane * 4) = 0u;
  bar();
  STAMP(2);
  f_prime<0>(lds, xa, wa, x, wf);
  tick_f<0>(lds, xa, wa, x, wf, acc);
  STAMP(30);
  vmwait<1>();
  if (lane == 0) {
    if (fold) __hip_atomic_store((GAS u64*)(p.ws + FL_P0 + ((j * 3 + w) * 16 + i) * FLS), tag, __ATOMIC_RELAXED, __HIP_MEMORY_SCOPE_AGENT);
    if (ffn && w == 3) __hip_atomic_store((GAS u64*)(p.ws + FL_C1 + (j * 16 + i) * FLS), tag, __ATOMIC_RELAXED, __HIP_MEMORY_SCOPE_AGENT);
  }
  tick_f<1>(lds, xa, wa, x, wf, acc);
  STAMP(31);
  const int token = 128 * i + 32 * w + l31;
  u4v sg = ld128(rs, token * 16, OFF_ST);
  tick_f<2>(lds, xa, wa, x, wf, acc);
  STAMP(32);
  tick_f<3>(lds, xa, wa, x, wf, acc);
  STAMP(33);
  float mean = __uint_as_float(sg[0]), sigma = __uint_as_float(sg[1]);
  if (!__all(sg[2] == (u32)tag && sg[3] == (u32)(tag >> 32))) stat_wait(p.ws, rs, token, tag, lane, mean, sigma);
  tick_f<4>(lds, xa, wa, x, wf, acc);
  STAMP(34);
  bf8 xaug;
  {
    u32 mh, ml, sh, sl;
    split2(-mean, mh, ml);
    split2(sigma, sh, sl);
    u4v r = {mh | (ml << 16), mh | (sh << 16), sl, 0u};
    if (hh) r = u4v{0u, 0u, 0u, 0u};
    xaug = as_bf8(r);
  }
  const float rstd = __builtin_amdgcn_rcpf(sigma);
  tick_f<5>(lds, xa, wa, x, wf, acc);
  STAMP(35);
  tick_f<6>(lds, xa, wa, x, wf, acc);
  STAMP(4);
  bar();
  STAMP(6);
  const u32 vl = lane * 16;
  if (ffn) {
#pragma unroll
    for (int n = 0; n < 4; ++n) acc[n] = mfma32(lds_frag(lds + L_AUG + ((n + w) & 3) * 1024 + vl), xaug, acc[n]);
    LAS char* hb = lds + L_HS + w * 8192 + vl;
#pragma unroll
    for (int n = 0; n < 4; ++n) {
#pragma unroll
      for (int r = 0; r < 16; r += 2) {
        const f2v t = {fmx(acc[n][r], 0.f), fmx(acc[n][r + 1], 0.f)};
        const f2v u = t * t;
        acc[n][r] = u[0];
        acc[n][r + 1] = u[1];
      }
#pragma unroll
      for (int s = 0; s < 2; ++s) *(LAS bf8*)(hb + (2 * ((n + w) & 3) + s) * 1024) = frag8(acc[n], s);
    }
  }
  bar();
  STAMP(5);
  f16v ak, av;
#pragma unroll
  for (int r = 0; r < 16; ++r) { ak[r] = 0.f; av[r] = 0.f; }
  {
    LAS const char* wb = lds + vl;
    auto kvo = [](int ks) -> u32 { return ks < 16 ? L_KVA + ks * 2048 : L_KVB + (ks - 16) * 2048; };
    auto kvo2 = [&](int ks) -> u32 { return ks < 28 ? kvo(ks) : L_AUG + 4 * 1024; };
    bf8 fk[4], fv[4];
#pragma unroll
    for (int ks = 0; ks < 3; ++ks) {
      fk[ks] = lds_frag(wb + kvo(ks));
      fv[ks] = lds_frag(wb + kvo(ks) + 1024);
    }
#pragma unroll
    for (int ks = 0; ks < 29; ++ks) {
      if (ks + 3 < 29) {
        fk[(ks + 3) & 3] = lds_frag(wb + kvo2(ks + 3));
        fv[(ks + 3) & 3] = lds_frag(wb + kvo2(ks + 3) + 1024);
      }
      const bf8 xb = ks < 28 ? x[(ks < 28 ? ks : 0) >> 2][ks & 3] : xaug;
      ak = mfma32(fk[ks & 3], xb, ak);
      av = mfma32(fv[ks & 3], xb, av);
      __builtin_amdgcn_sched_group_barrier(0x8, 1, 0);
      __builtin_amdgcn_sched_group_barrier(0x100, 1, 0);
      __builtin_amdgcn_sched_group_barrier(0x8, 1, 0);
      __builtin_amdgcn_sched_group_barrier(0x100, 1, 0);
      SCHED();
    }
  }
  cnt_arrive((u32)(size_t)lds + L_CNT + 4, lane);
  STAMP(7);
  const float cexp = rstd * LOG2E;
  {
    f16v ep, en;
    exp_pair(ak, cexp, ep, en);
    const float ap = rstd * __builtin_amdgcn_rcpf(sum28(ep, hh)), an = rstd * __builtin_amdgcn_rcpf(sum28(en, hh));
    const int row = 32 * w + l31;
    LAS char* base = lds + row * 64 + 8 * hh;
    const int key = (l31 >> 1) & 3;
#pragma unroll
    for (int q = 0; q < 4; ++q) {
      const u32 o = (q ^ key) << 4;
      *(LAS u2v*)(base + L_PHI + o) = u2v{pk2v(pair(ep, 4 * q) * ap), pk2v(pair(ep, 4 * q + 2) * ap)};
      *(LAS u2v*)(base + L_PHI + 8192 + o) = u2v{pk2v(pair(en, 4 * q) * an), pk2v(pair(en, 4 * q + 2) * an)};
      *(LAS u2v*)(base + L_V + o) = u2v{pk2(av[4 * q], av[4 * q + 1]), pk2(av[4 * q + 2], av[4 * q + 3])};
    }
  }
  STAMP(8);
  cnt_arrive((u32)(size_t)lds + L_CNT, lane);
  if (w < 2) {
    cnt_wait((u32)(size_t)lds + L_CNT);
    STAMP(9);
    const int q4 = (lane & 15) >> 2, pp = lane & 3, blk = (lane >> 4) & 1;
    u32 ta[2];
#pragma unroll
    for (int t = 0; t < 2; ++t) {
      const int row = 8 * hh + 4 * t + q4;
      ta[t] = row * 64 + (((2 * blk + (pp >> 1)) ^ ((row >> 1) & 3)) << 4) + 8 * (pp & 1);
    }
    f16v kv;
#pragma unroll
    for (int r = 0; r < 16; ++r) kv[r] = 0.f;
    LAS const char* pa = lds + L_PHI + w * 8192;
    LAS const char* pb = lds + L_V;
#pragma unroll
    for (int ks = 0; ks < 8; ++ks)
      kv = mfma32(lds_tr2(pa + ta[0] + ks * 1024, pa + ta[1] + ks * 1024), lds_tr2(pb + ta[0] + ks * 1024, pb + ta[1] + ks * 1024), kv);
    const u32 o = OFF_KVP + (j * 2 + w) * KVP_HS + i * 2048;
    st128(rs, vl, o, __builtin_bit_cast(u4v, frag8(kv, 0)));
    st128(rs, vl, o + 1024, __builtin_bit_cast(u4v, frag8(kv, 1)));
  }
  STAMP(18);
  cnt_wait((u32)(size_t)lds + L_CNT + 8);
  f16v aq;
#pragma unroll
  for (int r = 0; r < 16; ++r) aq[r] = 0.f;
  {
    LAS const char* wb = lds + L_Q + vl;
    bf8 fq[6];
#pragma unroll
    for (int ks = 0; ks < 5; ++ks) fq[ks] = lds_frag(wb + ks * 1024);
#pragma unroll
    for (int ks = 0; ks < 29; ++ks) {
      if (ks + 5 < 28) fq[(ks + 5) % 6] = lds_frag(wb + (ks + 5) * 1024);
      else if (ks + 5 == 28) fq[(ks + 5) % 6] = lds_frag(lds + L_AUG + 6 * 1024 + vl);
      aq = mfma32(fq[ks % 6], ks < 28 ? x[(ks < 28 ? ks : 0) >> 2][ks & 3] : xaug, aq);
      SCHED();
      if (ks == 15 && w < 2) flag_set(p.ws, FL_KV + ((j * 2 + w) * 16 + i) * FLS, tag, lane);
    }
  }
  STAMP(10);
  bf8 eq[2][2];
  auto qepi = [&]() {
    f16v ep, en;
    exp_pair(aq, cexp, ep, en);
    eq[0][0] = frag8(ep, 0); eq[0][1] = frag8(ep, 1);
    eq[1][0] = frag8(en, 0); eq[1][1] = frag8(en, 1);
  };
  const int m64 = R.m64, cc = R.cc;
  const int bm = w & 1, bn = w >> 1;
  u32 ha, wb;
  {
    const int q4 = (lane & 15) >> 2, pp = lane & 3, blk = (lane >> 4) & 1;
    ha = bm * 8192 + bn * 4096 + vl;
    wb = bn * 8192 + (4 * hh + q4) * 128 + (4 * (q4 >> 1) + 2 * blk + (pp >> 1)) * 16 + 8 * (pp & 1);
  }
  const u4v sg2 = ld128(rs, (64 * m64 + 32 * bm + l31) * 16, OFF_ST);
  const u32 b2 = *((const GAS u16*)p.b2 + cc * 64 + bn * 32 + l31);
  f16v g0, g1, a0, a1;
#pragma unroll
  for (int r = 0; r < 16; ++r) { a0[r] = 0.f; a1[r] = 0.f; }
  bf8 fh[4], f0[4], f1[4];
  u32 hp = 0, wp = 0;
  auto point = [&](int g) { hp = hslot(g) + ha; wp = (g == 16 ? L_PHI : wslot(g)) + wb; };
  auto req = [&](int ks) {
    fh[ks] = lds_frag(lds + hp + ks * 1024);
    f0[ks] = lds_tr2(lds + wp + ks * 2048, lds + wp + ks * 2048 + 1024);
    f1[ks] = lds_tr2(lds + (wp ^ 64) + ks * 2048, lds + (wp ^ 64) + ks * 2048 + 1024);
  };
  auto mm = [&](int ks) {
    a0 = mfma32(f0[ks], fh[ks], a0);
    a1 = mfma32(f1[ks], fh[ks], a1);
    __builtin_amdgcn_sched_group_barrier(0x8, 1, 0);
    __builtin_amdgcn_sched_group_barrier(0x100, 3, 0);
    __builtin_amdgcn_sched_group_barrier(0x8, 1, 0);
    __builtin_amdgcn_sched_group_barrier(0x100, 2, 0);
    SCHED();
  };
  auto tick = [&](int g) {
    req(3); mm(0);
    bar_only();
    point(g + 1);
    req(0); mm(1);
    req(1); mm(2);
    req(2); mm(3);
  };
  const u32 krflag = FL_KR + (j * 8 + min(lane, 7)) * FLS;
  int g = 0;
  u4v kt[4];
  auto oload = [&]() {
#pragma unroll
    for (int q = 0; q < 4; ++q) kt[q] = ld128(rs, vl, OFF_KVT + (j * 4 + q) * 1024);
  };
  auto ostore = [&]() {
    f16v np, nn;
#pragma unroll
    for (int r = 0; r < 16; ++r) { np[r] = 0.f; nn[r] = 0.f; }
    np = mfma32(as_bf8(kt[0]), eq[0][0], np);
    nn = mfma32(as_bf8(kt[2]), eq[1][0], nn);
    np = mfma32(as_bf8(kt[1]), eq[0][1], np);
    nn = mfma32(as_bf8(kt[3]), eq[1][1], nn);
    const float ia = __builtin_amdgcn_rcpf(xhalf_hi(np[13])), ib = __builtin_amdgcn_rcpf(xhalf_hi(nn[13]));
    const float den = xhalf_hi(np[12]) * ia + xhalf_hi(nn[12]) * ib + 1e-6f;
    const float id = __builtin_amdgcn_rcpf(den), fa = ia * id, fb = ib * id;
    u32 ov[8];
#pragma unroll
    for (int r = 0; r < 16; r += 2) ov[r >> 1] = pk2v(pair(np, r) * fa + pair(nn, r) * fb);
    if (hh) { ov[6] = 0u; ov[7] = 0u; }
    const u32 ob = OFF_O + (4 * i + w) * O_TILE + 2 * j * 1024;
    st128(rs, vl, ob, u4v{ov[0], ov[1], ov[2], ov[3]});
    st128(rs, vl, ob + 1024, u4v{ov[4], ov[5], ov[6], ov[7]});
  };
  const u32 oflag = FL_O + ((i * 16 + j) * 4 + w) * FLS;
  if (R.bwork) {
    bar();
    point(0);
    req(0); req(1); req(2);
    SCHED();
    tick(0);
    tick(1);
    g = 2;
    qepi();
    SCHED();
    u64 kr = 0;
    bool ready = false;
#pragma unroll 1
    while (g < 10) {
      tick(g);
      ++g;
      if (__all(kr == tag)) { ready = true; break; }
      if (g >= 4) kr = __hip_atomic_load((GAS u64*)(p.ws + krflag), __ATOMIC_RELAXED, __HIP_MEMORY_SCOPE_AGENT);
    }
    if (!ready) flag_wait(p.ws, krflag, 8, tag, lane, 6);
    STAMP(14);
    oload();
    tick(g); ++g;
    ostore();
    tick(g); ++g;
    flag_set(p.ws, oflag, tag, lane);
    STAMP(15);
  } else {
    const int hs = R.rho, sk = w & 1, hf = w >> 1;
    flag_wait(p.ws, FL_KV + (hs * 16 + lane) * FLS, 16, tag, lane, 5);
    STAMP(12);
    const u32 o = OFF_KVP + hs * KVP_HS + sk * 1024 + hf * 512;
    u4v t[8];
#pragma unroll
    for (int q = 0; q < 8; ++q) t[q] = ld128(rs, hh * 8 * 2048 + l31 * 16, o + q * 2048);
    f2v sm[4];
#pragma unroll
    for (int e = 0; e < 4; ++e) sm[e] = f2v{bf_lo(t[0][e]), bf_hi(t[0][e])};
#pragma unroll
    for (int q = 1; q < 8; ++q)
#pragma unroll
      for (int e = 0; e < 4; ++e) sm[e] += f2v{bf_lo(t[q][e]), bf_hi(t[q][e])};
    u4v r;
#pragma unroll
    for (int e = 0; e < 4; ++e) r[e] = pk2(xhalf_sum(sm[e][0]), xhalf_sum(sm[e][1]));
    if (l31 == 29) r = u4v{0x3f803f80u, 0x3f803f80u, 0x3f803f80u, 0x3f803f80u};
    if (l31 >= 30) r = u4v{0u, 0u, 0u, 0u};
    if (sk == 1 && hf == 1) { r[2] = 0u; r[3] = 0u; }
    if (lane < 32) st128(rs, l31 * 16, OFF_KVT + (hs * 2 + sk) * 1024 + hf * 512, r);
    flag_set(p.ws, FL_KR + ((hs * 2 + sk) * 2 + hf) * FLS, tag, lane);
    STAMP(13);
    qepi();
    flag_wait(p.ws, krflag, 8, tag, lane, 6);
    STAMP(14);
    oload();
    ostore();
    flag_set(p.ws, oflag, tag, lane);
    STAMP(15);
    return;
  }
#pragma unroll 1
  for (; g < 13; ++g) tick(g);
  req(3); mm(0); mm(1); mm(2); mm(3);
  STAMP(24);
  g0 = a0; g1 = a1;
#pragma unroll
  for (int r = 0; r < 16; ++r) { a0[r] = 0.f; a1[r] = 0.f; }
  bar();
  point(14);
  req(0); req(1); req(2);
  SCHED();
#pragma unroll 1
  for (g = 14; g < 17; ++g) tick(g);
  req(3); mm(0); mm(1); mm(2); mm(3);
  f16v ag, accb;
  {
    LAS char* dst = lds + (bm * 2 + (1 - bn)) * 8192 + vl;
    LAS const char* src = lds + (bm * 2 + bn) * 8192 + vl;
#pragma unroll
    for (int q = 0; q < 4; ++q) {
      const f16v& gs = bn ? g0 : g1;
      const f16v& as = bn ? a0 : a1;
      *(LAS f4v*)(dst + q * 1024) = f4v{gs[4 * q], gs[4 * q + 1], gs[4 * q + 2], gs[4 * q + 3]};
      *(LAS f4v*)(dst + 4096 + q * 1024) = f4v{as[4 * q], as[4 * q + 1], as[4 * q + 2], as[4 * q + 3]};
    }
    bar();
#pragma unroll
    for (int q = 0; q < 4; ++q) {
      const f4v gv = *(LAS const f4v*)(src + q * 1024), sv = *(LAS const f4v*)(src + 4096 + q * 1024);
#pragma unroll
      for (int e = 0; e < 4; ++e) {
        ag[4 * q + e] = (bn ? g1[4 * q + e] : g0[4 * q + e]) + gv[e];
        accb[4 * q + e] = (bn ? a1[4 * q + e] : a0[4 * q + e]) + sv[e];
      }
    }
  }
  STAMP(16);
  {
    float m2 = __uint_as_float(sg2[0]), s2 = __uint_as_float(sg2[1]);
    if (!__all(sg2[2] == (u32)tag && sg2[3] == (u32)(tag >> 32))) stat_wait(p.ws, rs, 64 * m64 + 32 * bm + l31, tag, lane, m2, s2);
    const float var = s2 * s2;
    u32 vh, vlo;
    split2(var, vh, vlo);
    u4v xa2 = {vh | (vlo << 16), 0u, 0u, 0u};
    u4v wa3 = {b2 | (b2 << 16), 0u, 0u, 0u};
    if (hh) { xa2[0] = 0u; wa3[0] = 0u; }
    ag = mfma32(as_bf8(wa3), as_bf8(xa2), ag);
    const float r2 = __builtin_amdgcn_rcpf(var);
    const int row = 32 * bm + l31;
    LAS char* base = lds + L_V + row * 128 + 8 * hh;
    const int key = (row >> 1) & 7;
#pragma unroll
    for (int q = 0; q < 4; ++q) {
      LAS u2v* ptr = (LAS u2v*)(base + (((4 * bn + q) ^ key) << 4));
      const u2v xv = *ptr;
      float o[4];
#pragma unroll
      for (int e = 0; e < 4; ++e) {
        const float gp = ag[4 * q + e] * r2;
        const float sg = gp * __builtin_amdgcn_rcpf(1.f + __builtin_amdgcn_exp2f(-gp * LOG2E));
        const float xr = (e & 1) ? bf_hi(xv[e >> 1]) : bf_lo(xv[e >> 1]);
        o[e] = __builtin_fmaf(accb[4 * q + e], sg, xr);
      }
      *ptr = u2v{pk2(o[0], o[1]), pk2(o[2], o[3])};
    }
  }
  bar();
  {
    const int row = 8 * wave + (lane >> 3);
    u4v v = *(LAS const u4v*)(lds + L_V + row * 128 + (((lane & 7) ^ ((row >> 1) & 7)) << 4));
    *(GAS u4v*)((char*)p.out + (size_t)(64 * m64 + row) * XROW + cc * 128 + (lane & 7) * 16) = v;
  }
  STAMP(17);
  DIAG_WAIT();
  STAMPC(63);
}

__global__ __launch_bounds__(512, 2) void fused_kernel(Params p) {
  extern __shared__ __attribute__((aligned(16))) char smem[];
  LAS char* lds = (LAS char*)smem;
  const int lane = threadIdx.x & 63;
  const int wave = __builtin_amdgcn_readfirstlane(threadIdx.x >> 6);
  const int b = blockIdx.x, chi = b & 7, sidx = b >> 3;
  Role R;
  R.i = 8 * (chi & 1) + (sidx & 7);
  R.j = 4 * (chi >> 1) + (sidx >> 3);
  R.jw = min(R.j, 13);
  R.bwork = sidx < 28;
  R.tq = R.bwork ? sidx / 7 : 0;
  R.cc = R.bwork ? sidx % 7 : 0;
  R.m64 = 4 * chi + R.tq;
  R.rho = 4 * chi + (sidx - 28);
  R.sidx = sidx;
  DIAG_INIT();
  STAMP(0);
  DIAG_WAIT();
  STAMPC(62);
  DIAG_WAIT();
  if (wave >= 4) loader_main(p, (u32)(size_t)lds, R, wave - 4, lane, wave);
  else compute_main(p, lds, R, wave, lane, wave);
  DIAG_DUMP();
}

extern "C" void kernel_launch(void* const* d_in, const int* in_sizes, int n_in, void* d_out, int out_size, void* d_ws,
                              size_t ws_size, hipStream_t stream) {
  Params p;
  p.x = (const u16*)d_in[0];
  p.wqkv = (const u16*)d_in[1];
  p.wphi = (const u16*)d_in[2];
  p.wo = (const u16*)d_in[3];
  p.w1 = (const u16*)d_in[5];
  p.b1 = (const u16*)d_in[6];
  p.w2 = (const u16*)d_in[7];
  p.b2 = (const u16*)d_in[8];
  p.out = (u16*)d_out;
  p.ws = (char*)d_ws;
  (void)hipFuncSetAttribute((const void*)fused_kernel, hipFuncAttributeMaxDynamicSharedMemorySize, LDS_BYTES);
  fused_kernel<<<256, 512, LDS_BYTES, stream>>>(p);
}
```

```cpp
#define DIAG
#include <stdio.h>
#define TOUCH_POS 1
#include <hip/hip_runtime.h>
#include <hip/hip_bf16.h>
#include <stdint.h>

typedef unsigned short u16;
typedef uint32_t u32;
typedef uint64_t u64;
typedef __bf16 bf2 __attribute__((ext_vector_type(2)));
typedef __bf16 bf4 __attribute__((ext_vector_type(4)));
typedef __bf16 bf8 __attribute__((ext_vector_type(8)));
typedef float f4v __attribute__((ext_vector_type(4)));
typedef float f16v __attribute__((ext_vector_type(16)));
typedef u32 u2v __attribute__((ext_vector_type(2)));
typedef u32 u4v __attribute__((ext_vector_type(4)));
#define LAS __attribute__((address_space(3)))
#define GAS __attribute__((address_space(1)))
#define CAS __attribute__((address_space(4)))
#define DI __device__ __forceinline__

constexpr int S_ = 2048, D_ = 448, NH = 16, DH = 28, FF = 1792;
constexpr int XROW = D_ * 2;
constexpr int QROW = 3 * D_ * 2;
constexpr int W1ROW = FF * 2;

constexpr u32 WS_EPOCH = 0, WS_ERR = 128;
constexpr u32 FL0 = 4096, FLS = 128;
constexpr u32 FL_P0 = FL0;
constexpr u32 FL_C1 = FL_P0 + 768 * FLS;
constexpr u32 FL_H = FL_C1 + 1024 * FLS;
constexpr u32 FL_KV = FL_H + 1024 * FLS;
constexpr u32 FL_KR = FL_KV + 512 * FLS;
constexpr u32 FL_O = FL_KR + 128 * FLS;
constexpr u32 OFF_ST = FL_O + 1024 * FLS;
constexpr u32 OFF_WP = OFF_ST + S_ * 16;
constexpr u32 WP_HEAD = 84 * 1024, WP_Q = 56 * 1024;
constexpr u32 OFF_CSP = OFF_WP + NH * WP_HEAD;
constexpr u32 OFF_C1P = OFF_CSP + NH * 3 * 2048;
constexpr u32 H_TILE = 112 * 1024 + 128, KVP_HS = 16 * 2048 + 128, O_TILE = 32 * 1024 + 128;
constexpr u32 OFF_H = OFF_C1P + 256 * 512;
constexpr u32 OFF_KVP = OFF_H + 64 * H_TILE;
constexpr u32 OFF_KVT = OFF_KVP + NH * 2 * KVP_HS;
constexpr u32 OFF_O = OFF_KVT + NH * 4 * 1024;
constexpr u32 OFF_DIAG = OFF_O + 64 * O_TILE;
constexpr u32 WS_USED = OFF_DIAG + 256 * 8 * 64 * 8;

constexpr u32 SLOT = 16384;
constexpr u32 L_PHI = 8 * SLOT;
constexpr u32 L_V = L_PHI + 16384;
constexpr u32 L_AUG = L_V + 8192;
constexpr u32 L_DUMMY = L_AUG + 7168 + 512;
constexpr u32 L_CNT = L_DUMMY + 256;
constexpr u32 LDS_BYTES = 163840;

constexpr float LOG2E = 1.4426950408889634f;
constexpr u32 SPIN_CAP = 1u << 18;

struct Params {
  const u16 *x, *wqkv, *wphi, *wo, *w1, *b1, *w2, *b2;
  u16* out;
  char* ws;
};

constexpr unsigned L_DIAG = 162816;
#define STAMP_(id, ins)                                                                               \
  do {                                                                                                \
    if ((wave & 3) == 0) {                                                                            \
      u32 a_, b_;                                                                                     \
      asm volatile("v_mov_b32 %0, s92\n\tv_mov_b32 %1, s96\n\tds_write_b32 %1, %0\n\ts_mov_b32 s96, %2\n\t" ins " s[92:93]"   \
                   : "=&v"(a_), "=&v"(b_) : "s"(L_DIAG + ((wave >> 2) * 64 + (id)) * 4) : "s92", "s93", "s96", "memory");      \
    }                                                                                                 \
  } while (0)
#define STAMP(id) STAMP_(id, "s_memrealtime")
#define STAMPC(id) STAMP_(id, "s_memtime")
#define DIAG_WAIT() asm volatile("s_waitcnt lgkmcnt(0)" ::: "memory")
#define DIAG_INIT() do { if ((wave & 3) == 0) { *(LAS volatile u32*)(size_t)(L_DIAG + ((wave >> 2) * 64 + lane) * 4) = 0u;      \
    asm volatile("s_waitcnt lgkmcnt(0)\n\ts_mov_b32 s96, %0" :: "s"(L_DIAG + ((wave >> 2) * 64 + 60) * 4) : "s96", "memory"); } } while (0)
#define DIAG_DUMP()                                                                                   \
  do {                                                                                                \
    if ((wave & 3) == 0) {                                                                            \
      asm volatile("s_waitcnt lgkmcnt(0)" ::: "memory");                                              \
      STAMP(60);                                                                                      \
      asm volatile("s_waitcnt lgkmcnt(0)" ::: "memory");                                              \
      ((GAS u32*)(p.ws + OFF_DIAG))[(blockIdx.x * 2 + (wave >> 2)) * 64 + lane] = *(LAS volatile u32*)(size_t)(L_DIAG + ((wave >> 2) * 64 + lane) * 4); \
    }                                                                                                 \
  } while (0)

DI float bf_lo(u32 u) { return __uint_as_float(u << 16); }
DI float bf_hi(u32 u) { return __uint_as_float(u & 0xffff0000u); }
DI u32 pk2(float a, float b) {
  bf2 v = {(__bf16)a, (__bf16)b};
  return __builtin_bit_cast(u32, v);
}
DI u32 bf_rn(float a) { return pk2(a, 0.f) & 0xffffu; }
DI bf8 as_bf8(u4v v) { return __builtin_bit_cast(bf8, v); }
DI bf8 frag8(const f16v& a, int s) {
  u4v r = {pk2(a[8 * s], a[8 * s + 1]), pk2(a[8 * s + 2], a[8 * s + 3]), pk2(a[8 * s + 4], a[8 * s + 5]), pk2(a[8 * s + 6], a[8 * s + 7])};
  return as_bf8(r);
}
DI f16v mfma32(bf8 a, bf8 b, f16v c) { return __builtin_amdgcn_mfma_f32_32x32x16_bf16(a, b, c, 0, 0, 0); }
DI f4v mfma16(bf8 a, bf8 b, f4v c) { return __builtin_amdgcn_mfma_f32_16x16x32_bf16(a, b, c, 0, 0, 0); }
DI bf8 lds_frag(LAS const char* p) { return *(LAS const bf8*)p; }
DI bf8 lds_tr2(LAS const char* p0, LAS const char* p1) {
  bf4 a = __builtin_amdgcn_ds_read_tr16_b64_v4bf16((LAS bf4*)p0);
  bf4 b = __builtin_amdgcn_ds_read_tr16_b64_v4bf16((LAS bf4*)p1);
  return __builtin_shufflevector(a, b, 0, 1, 2, 3, 4, 5, 6, 7);
}
DI float xhalf_max(float v) {
  auto r = __builtin_amdgcn_permlane32_swap(__float_as_uint(v), __float_as_uint(v), false, false);
  return __builtin_elementwise_maximum(__uint_as_float(r[0]), __uint_as_float(r[1]));
}
DI float xhalf_min(float v) {
  auto r = __builtin_amdgcn_permlane32_swap(__float_as_uint(v), __float_as_uint(v), false, false);
  return __builtin_elementwise_minimum(__uint_as_float(r[0]), __uint_as_float(r[1]));
}
DI float xhalf_sum(float v) {
  auto r = __builtin_amdgcn_permlane32_swap(__float_as_uint(v), __float_as_uint(v), false, false);
  return __uint_as_float(r[0]) + __uint_as_float(r[1]);
}
DI float xhalf_hi(float v) {
  auto r = __builtin_amdgcn_permlane32_swap(__float_as_uint(v), __float_as_uint(v), false, false);
  return __uint_as_float(r[1]);
}
DI float dpp_ror(float v, int n) {
  const int iv = __builtin_bit_cast(int, v);
  int r;
  switch (n) {
    case 8: r = __builtin_amdgcn_update_dpp(0, iv, 0x128, 0xf, 0xf, false); break;
    case 4: r = __builtin_amdgcn_update_dpp(0, iv, 0x124, 0xf, 0xf, false); break;
    case 2: r = __builtin_amdgcn_update_dpp(0, iv, 0x122, 0xf, 0xf, false); break;
    default: r = __builtin_amdgcn_update_dpp(0, iv, 0x121, 0xf, 0xf, false); break;
  }
  return __builtin_bit_cast(float, r);
}
DI float half_sum(float v) {
  v += dpp_ror(v, 8); v += dpp_ror(v, 4); v += dpp_ror(v, 2); v += dpp_ror(v, 1);
  auto r = __builtin_amdgcn_permlane16_swap(__float_as_uint(v), __float_as_uint(v), false, false);
  return __uint_as_float(r[0]) + __uint_as_float(r[1]);
}
typedef float f2v __attribute__((ext_vector_type(2)));
DI f2v pair(const f16v& a, int r) { return f2v{a[r], a[r + 1]}; }
DI u32 pk2v(f2v v) { return pk2(v[0], v[1]); }
DI void bar() { asm volatile("s_waitcnt lgkmcnt(0)\n\ts_barrier" ::: "memory"); }
DI void bar_only() { asm volatile("s_barrier" ::: "memory"); }
template <int N> DI void vmwait() { asm volatile("s_waitcnt vmcnt(%0)" ::"i"(N) : "memory"); }
#define SCHED() __builtin_amdgcn_sched_barrier(0)

DI void dma(u32 voff, const void* sbase, u32 ldsaddr) {
  asm volatile("s_mov_b32 m0, %2\n\ts_nop 0\n\tglobal_load_lds_dwordx4 %0, %1" ::"v"(voff), "s"(sbase), "s"(ldsaddr) : "memory");
}
DI void dma_sc1(u32 voff, const void* sbase, u32 ldsaddr) {
  asm volatile("s_mov_b32 m0, %2\n\ts_nop 0\n\tglobal_load_lds_dwordx4 %0, %1 sc1" ::"v"(voff), "s"(sbase), "s"(ldsaddr) : "memory");
}

DI void dma4(u32 v0, u32 v1, u32 v2, u32 v3, const void* sbase, u32 ldsaddr) {
  asm volatile("s_mov_b32 m0, %5\n\ts_nop 0\n\tglobal_load_lds_dwordx4 %0, %4\n\tglobal_load_lds_dwordx4 %1, %4 offset:1024\n\t"
               "global_load_lds_dwordx4 %2, %4 offset:2048\n\tglobal_load_lds_dwordx4 %3, %4 offset:3072"
               ::"v"(v0), "v"(v1), "v"(v2), "v"(v3), "s"(sbase), "s"(ldsaddr) : "memory");
}
template <int N> DI void dma_lin(u32 v, const void* sbase, u32 ldsaddr) {
  if constexpr (N == 1)
    asm volatile("s_mov_b32 m0, %2\n\ts_nop 0\n\tglobal_load_lds_dwordx4 %0, %1 sc1" ::"v"(v), "s"(sbase), "s"(ldsaddr) : "memory");
  else if constexpr (N == 2)
    asm volatile("s_mov_b32 m0, %2\n\ts_nop 0\n\tglobal_load_lds_dwordx4 %0, %1 sc1\n\tglobal_load_lds_dwordx4 %0, %1 offset:1024 sc1"
                 ::"v"(v), "s"(sbase), "s"(ldsaddr) : "memory");
  else
    asm volatile("s_mov_b32 m0, %2\n\ts_nop 0\n\tglobal_load_lds_dwordx4 %0, %1 sc1\n\tglobal_load_lds_dwordx4 %0, %1 offset:1024 sc1\n\t"
                 "global_load_lds_dwordx4 %0, %1 offset:2048 sc1\n\tglobal_load_lds_dwordx4 %0, %1 offset:3072 sc1"
                 ::"v"(v), "s"(sbase), "s"(ldsaddr) : "memory");
}

typedef __amdgpu_buffer_rsrc_t rsrc_t;
DI u4v ld128(rsrc_t r, u32 voff, u32 soff) { return __builtin_amdgcn_raw_buffer_load_b128(r, voff, soff, 16); }
DI u32 ld32(rsrc_t r, u32 voff, u32 soff) { return __builtin_amdgcn_raw_buffer_load_b32(r, voff, soff, 16); }
DI void st128(rsrc_t r, u32 voff, u32 soff, u4v v) { __builtin_amdgcn_raw_buffer_store_b128(v, r, voff, soff, 16); }
DI void st64(rsrc_t r, u32 voff, u32 soff, u2v v) { __builtin_amdgcn_raw_buffer_store_b64(v, r, voff, soff, 16); }
DI void st32(rsrc_t r, u32 voff, u32 soff, u32 v) { __builtin_amdgcn_raw_buffer_store_b32(v, r, voff, soff, 16); }
DI float fmx(float a, float b) { return __builtin_elementwise_maximum(a, b); }
DI float fmn(float a, float b) { return __builtin_elementwise_minimum(a, b); }
DI void touch(u32 voff, const void* sbase, u32 lb) {
  asm volatile("s_mov_b32 m0, %2\n\ts_nop 0\n\tglobal_load_lds_dword %0, %1" ::"v"(voff), "s"(sbase), "s"(lb + L_DUMMY) : "memory");
}

DI void cnt_arrive(u32 addr, int lane) {
  asm volatile("s_waitcnt lgkmcnt(0)" ::: "memory");
  if (lane == 0) __hip_atomic_fetch_add((LAS u32*)(size_t)addr, 1u, __ATOMIC_RELAXED, __HIP_MEMORY_SCOPE_WORKGROUP);
}
DI void cnt_wait(u32 addr) {
#pragma unroll 1
  for (u32 spin = 0; spin < SPIN_CAP; ++spin)
    if (__builtin_amdgcn_readfirstlane(__hip_atomic_load((LAS u32*)(size_t)addr, __ATOMIC_RELAXED, __HIP_MEMORY_SCOPE_WORKGROUP)) >= 4u) break;
  asm volatile("" ::: "memory");
}
DI u64 tag64(u32 t) { return (u64)t | ((u64)(t * 0x9E3779B1u + 0x7F4A7C15u) << 32); }

DI void flag_set(char* ws, u32 off, u64 tag, int lane) {
  asm volatile("s_waitcnt vmcnt(0)" ::: "memory");
  if (lane == 0) __hip_atomic_store((GAS u64*)(ws + off), tag, __ATOMIC_RELAXED, __HIP_MEMORY_SCOPE_AGENT);
}
DI void flag_wait(char* ws, u32 off, int n, u64 tag, int lane, u32 code) {
  GAS u64* ptr = (GAS u64*)(ws + off);
#pragma unroll 1
  for (u32 spin = 0;; ++spin) {
    u64 v = tag;
    if (lane < n) v = __hip_atomic_load(ptr, __ATOMIC_RELAXED, __HIP_MEMORY_SCOPE_AGENT);
    if (__all(v == tag)) break;
    if (spin > SPIN_CAP) {
      if (lane == 0) __hip_atomic_store((GAS u32*)(ws + WS_ERR), code, __ATOMIC_RELAXED, __HIP_MEMORY_SCOPE_AGENT);
      break;
    }
    __builtin_amdgcn_s_sleep(1);
  }
  asm volatile("" ::: "memory");
}
DI void stat_wait(char* ws, rsrc_t rs, int token, u64 tag, int lane, float& mean, float& sigma) {
#pragma unroll 1
  for (u32 spin = 0;; ++spin) {
    u4v g = ld128(rs, token * 16, OFF_ST);
    mean = __uint_as_float(g[0]);
    sigma = __uint_as_float(g[1]);
    bool ok = g[2] == (u32)tag && g[3] == (u32)(tag >> 32);
    if (__all(ok)) break;
    if (spin > SPIN_CAP) {
      if (lane == 0) __hip_atomic_store((GAS u32*)(ws + WS_ERR), 99u, __ATOMIC_RELAXED, __HIP_MEMORY_SCOPE_AGENT);
      break;
    }
    __builtin_amdgcn_s_sleep(1);
  }
}
DI void split2(float v, u32& hi, u32& lo) {
  hi = bf_rn(v);
  lo = bf_rn(v - __uint_as_float(hi << 16));
}
DI bf8 aug_w(float cs, u32 beta, int hh) {
  u32 hi, lo;
  split2(cs, hi, lo);
  u4v r = {hi | (hi << 16), lo | (beta << 16), beta, 0u};
  if (hh) r = u4v{0u, 0u, 0u, 0u};
  return as_bf8(r);
}

struct Role {
  int i, j;
  int jw;
  int m64, cc;
  int tq;
  bool bwork;
  int rho;
  int sidx;
};

DI u32 wslot(int g) { return (u32)((0x1076u >> (4 * (g & 3))) & 15) * SLOT; }
DI u32 hslot(int g) { return (u32)((0x3254u >> (4 * (g & 3))) & 15) * SLOT; }
DI constexpr u32 fslot(int c) { return (u32)(2 * (c % 4)) * SLOT; }
constexpr u32 L_KVA = 6 * SLOT;
constexpr u32 L_KVB = 0;
constexpr u32 L_Q = 2 * SLOT;
constexpr u32 L_HS = 4 * SLOT;

DI void loader_main(const Params& p, u32 lb, const Role& R, int lw, int lane, int wave) {
  const int i = R.i, j = R.j, m64 = R.m64, cc = R.cc;
  const u32 vx0 = (lane >> 3) * XROW + (((lane & 7) ^ (lane >> 4)) << 4);
  const u32 vx1 = (vx0 ^ 64) + (8 * XROW - 1024), vx2 = vx0 + 2 * (8 * XROW - 1024), vx3 = vx1 + 2 * (8 * XROW - 1024);
  const u32 vw0 = (lane >> 4) * W1ROW + (((lane & 15) ^ ((lane >> 4) << 2)) << 4);
  const u32 vw1 = vw0 + (4 * W1ROW - 1024), vw2 = vw0 + 2 * (4 * W1ROW - 1024), vw3 = vw0 + 3 * (4 * W1ROW - 1024);
  const char* xb = (const char*)p.x + (size_t)(128 * i + 32 * lw) * XROW;
  const char* w1b = (const char*)p.w1 + (size_t)(16 * lw) * W1ROW + R.jw * 256;
  const u32 lq = lb + lw * 4096;
  int gc = 0;
  auto stage = [&](u32 slot) {
    dma4(vx0, vx1, vx2, vx3, xb + gc * 128, lq + slot);
    dma4(vw0, vw1, vw2, vw3, w1b + (size_t)gc * (64 * W1ROW), lq + slot + SLOT);
    ++gc;
  };
  if (lw == 3) {
    const int t = R.sidx * 6 + min(lane, 5);
    const char* q;
    if (t < 28) q = (const char*)p.x + t * 65536;
    else if (t < 47) q = (const char*)p.wqkv + (t - 28) * 63000;
    else if (t < 48) q = (const char*)p.wphi;
    else if (t < 54) q = (const char*)p.wo + (t - 48) * 65536;
    else if (t < 79) q = (const char*)p.w1 + (t - 54) * 64000;
    else if (t < 80) q = (const char*)p.b1;
    else if (t < 105) q = (const char*)p.w2 + (t - 80) * 64000;
    else if (t < 106) q = (const char*)p.b2;
    else if (t < 134) q = (const char*)p.out + (t - 106) * 65536;
    else if (t < 166) q = p.ws + OFF_H + (size_t)(2 * (t - 134) + 1) * H_TILE - 128;
    else if (t < 174) q = p.ws + OFF_KVP + (size_t)(4 * (t - 166) + 1) * KVP_HS - 128;
    else if (t < 182) q = p.ws + OFF_O + (size_t)(8 * (t - 174) + 1) * O_TILE - 128;
    else q = p.ws + 512;
    asm volatile("s_mov_b32 m0, %1\n\ts_nop 0\n\tglobal_load_lds_dword %0, off" ::"v"(q), "s"(lb + L_DUMMY) : "memory");
  }
  auto warm = [&](int c0) {
    const int lx = min(lane, 8 * (7 - c0) - 1), lz = min(lane, 4 * (7 - c0) - 1);
    touch((32 * (j & 3) + 8 * lw + (lx & 7)) * XROW + (c0 + (lx >> 3)) * 128, (const char*)p.x + (size_t)(128 * i) * XROW, lb);
    touch((64 * (c0 + (lz >> 2)) + 8 * (i & 7) + 2 * lw + ((lz & 3) >> 1)) * W1ROW + (lz & 1) * 128, (const char*)p.w1 + R.jw * 256, lb);
  };
  bar_only();
  STAMP(12);
  stage(fslot(0));
  STAMP(13);
  warm(1);
  stage(fslot(1));
  const u32 E = *(CAS const u32*)(p.ws + WS_EPOCH);
  const u64 tag = tag64(E + 1);
  const rsrc_t rs = __builtin_amdgcn_make_buffer_rsrc(p.ws, 0, WS_USED, 0x00020000);
  const u32 vl = lane * 16;
  const u32 bchunk = ((lane & 7) ^ (((lane >> 4) & 1) << 2)) << 4;
  const u32 vb0 = (lane >> 3) * XROW + bchunk;
  const u32 vb1 = vb0 + (8 * XROW - 1024), vb2 = vb0 + 2 * (8 * XROW - 1024), vb3 = vb0 + 3 * (8 * XROW - 1024);
  const u32 vb3c = min(lane >> 3, 3) * XROW + bchunk + 3 * (8 * XROW - 1024);
  STAMP(1);
  vmwait<10>(); bar_only();
  STAMP(2);
  stage(fslot(2)); vmwait<8>(); bar_only();
  stage(fslot(3)); vmwait<8>(); bar_only();
  stage(fslot(4)); vmwait<8>(); bar_only();
  stage(fslot(5)); vmwait<8>(); bar_only();
  stage(fslot(6));
  const char* wp = p.ws + OFF_WP + (size_t)j * WP_HEAD;
  const u32 pflag = lane < 42 ? FL_P0 + (j * 48 + (lane / 14) * 16 + lane % 14) * FLS : FL_C1 + (R.jw * 16 + min(lane - 42, 15)) * FLS;
  dma_sc1(pflag, p.ws, lb + L_V + 4096 + lw * 1024);
  vmwait<9>(); bar_only();
  STAMP(3);
  vmwait<0>();
  {
    const u2v f = *(LAS const u2v*)(size_t)(lb + L_V + 4096 + lw * 1024 + lane * 16);
    if (!__all(f[0] == (u32)tag && f[1] == (u32)(tag >> 32))) flag_wait(p.ws, pflag, 58, tag, lane, 1);
  }
  STAMP(4);
  bar_only();
  const u32 gs = lb + L_PHI + lw * 4096;
  {
    const char* c1 = p.ws + OFF_C1P + (size_t)R.jw * 16 * 512 + lw * 128;
    const u32 vg = (lane >> 3) * 512 + (lane & 7) * 16;
    dma_lin<1>(vg, c1, gs);
    dma_lin<1>(vg, c1 + 8 * 512, gs + 1024);
    dma_lin<2>(vl, p.ws + OFF_CSP + (size_t)(j * 3 + min(lw, 2)) * 2048, gs + 2048);
  }
  asm volatile("s_mov_b32 m0, %2\n\ts_nop 0\n\tglobal_load_lds_dword %0, %1" ::"v"(min(lane, 15) * 4), "s"((const char*)p.b1 + (R.jw * 128 + lw * 32) * 2),
               "s"(lb + L_V + lw * 256) : "memory");
#pragma unroll
  for (int c = 0; c < 2; ++c) dma_lin<2>(vl, wp + c * 8192 + lw * 2048, lb + L_KVA + c * 8192 + lw * 2048);
  vmwait<4>();
  {
    const int hh = lane >> 5, l31 = lane & 31;
    LAS const float* src = (LAS const float*)(size_t)(gs + hh * 1024 + l31 * 4);
    float a = 0.f, b = 0.f;
#pragma unroll
    for (int q = 0; q < 8; ++q) a += src[32 * q];
    LAS const float* src2 = (LAS const float*)(size_t)(gs + 2048 + hh * 7 * 128 + l31 * 4);
#pragma unroll
    for (int q = 0; q < 7; ++q) b += src2[32 * q];
    const u32 beta1 = *(LAS const u16*)(size_t)(lb + L_V + lw * 256 + l31 * 2);
    LAS char* aug = (LAS char*)(size_t)(lb + L_AUG) + vl;
    *(LAS bf8*)(aug + lw * 1024) = aug_w(xhalf_sum(a), beta1, hh);
    if (lw < 3) *(LAS bf8*)(aug + (4 + lw) * 1024) = aug_w(xhalf_sum(b), (lw == 1 && l31 == 28) ? 0x3f80u : 0u, hh);
    asm volatile("s_waitcnt lgkmcnt(0)" ::: "memory");
  }
  bar_only();
  STAMP(5);
#pragma unroll
  for (int c = 2; c < 4; ++c) dma_lin<2>(vl, wp + c * 8192 + lw * 2048, lb + L_KVA + c * 8192 + lw * 2048);
#pragma unroll
  for (int c = 4; c < 7; ++c) dma_lin<2>(vl, wp + c * 8192 + lw * 2048, lb + L_KVB + (c - 4) * 8192 + lw * 2048);
#pragma unroll
  for (int c = 0; c < 7; ++c) dma_lin<1>(vl, wp + WP_Q + c * 4096 + lw * 1024, lb + L_Q + c * 4096 + lw * 1024);
  if (R.bwork) {
#pragma unroll 1
    for (int q = lw; q < 9; q += 4) {
      if (q < 7) touch(lane * XROW, (const char*)p.w2 + (size_t)(448 * R.tq + 64 * q) * XROW + cc * 128, lb);
      else touch(min(lane, q == 7 ? 63 : 47) * XROW, (const char*)p.wo + (size_t)(112 * R.tq + 64 * (q - 7)) * XROW + cc * 128, lb);
    }
    if (lw == 0) vmwait<10>(); else vmwait<9>();
  } else {
    vmwait<7>();
  }
  bar_only();
  if (j < 14) {
    LAS const char* src = (LAS const char*)(size_t)(lb + L_HS + lw * 8192) + vl;
    const u32 hb = OFF_H + (4 * i + lw) * H_TILE + 8 * j * 1024;
    u4v hv[8];
#pragma unroll
    for (int q = 0; q < 8; ++q) hv[q] = *(LAS const u4v*)(src + q * 1024);
#pragma unroll
    for (int q = 0; q < 8; ++q) st128(rs, vl, hb + q * 1024, hv[q]);
  }
  STAMP(14);
  vmwait<0>();
  if (j < 14 && lane == 0) __hip_atomic_store((GAS u64*)(p.ws + FL_H + ((i * 16 + j) * 4 + lw) * FLS), tag, __ATOMIC_RELAXED, __HIP_MEMORY_SCOPE_AGENT);
  cnt_arrive(lb + L_CNT + 8, lane);
  cnt_wait(lb + L_CNT + 4);
  STAMP(6);
  const char* w2p = (const char*)p.w2 + (size_t)(32 * lw) * XROW + cc * 128;
  auto issue_w = [&](int g) {
    if (g < 14) {
      dma4(vb0, vb1, vb2, vb3, w2p, lq + wslot(g));
      w2p += 128 * XROW;
    } else {
      dma4(vb0, vb1, vb2, vb3c, (const char*)p.wo + (size_t)(28 * (4 * (g - 14) + lw)) * XROW + cc * 128, lq + wslot(g));
    }
  };
  const char* hp = p.ws + OFF_H + (size_t)(2 * m64 + (lw >> 1)) * H_TILE + (lw & 1) * 4096;
  auto issue_h = [&](int g) {
    if (g == 14) hp = p.ws + OFF_O + (size_t)(2 * m64 + (lw >> 1)) * O_TILE + (lw & 1) * 4096;
    dma_lin<4>(vl, hp, lq + hslot(g));
    hp += 8192;
  };
  if (!R.bwork) return;
  issue_w(0);
  issue_w(1);
  issue_w(2);
  issue_w(3);
  flag_wait(p.ws, FL_H + (((m64 >> 1) * 16 + (lane >> 1)) * 4 + 2 * (m64 & 1) + (lane & 1)) * FLS, 28, tag, lane, 2);
  STAMP(7);
  cnt_wait(lb + L_CNT + 8);
  issue_h(0);
  touch(lane * 128, p.ws + OFF_H + (size_t)(2 * m64 + (lw >> 1)) * H_TILE + (16 * cc + 8 * (lw & 1)) * 1024, lb);
  issue_h(1);
  vmwait<5>(); bar_only();
  issue_h(2);
  vmwait<4>(); bar_only();
  issue_h(3);
  {
    const char* b = (const char*)p.x + (size_t)(64 * m64 + 16 * lw) * XROW + cc * 128;
    dma(vx0, b, lb + L_V + lw * 2048);
    dma(vx0 ^ 64, b + 8 * XROW, lb + L_V + lw * 2048 + 1024);
  }
  STAMP(8);
  vmwait<6>(); bar_only();
#pragma unroll 1
  for (int g = 2; g < 12; ++g) {
    issue_h(g + 2);
    issue_w(g + 2);
    vmwait<8>();
    bar_only();
  }
  issue_w(14);
  vmwait<4>(); bar_only();
  issue_w(15);
  dma4(vb0, vb1, vb2, vb3c, (const char*)p.wo + (size_t)(28 * (8 + lw)) * XROW + cc * 128, lb + L_PHI + lw * 4096);
  flag_wait(p.ws, FL_O + (((m64 >> 1) * 16 + (lane >> 1)) * 4 + 2 * (m64 & 1) + (lane & 1)) * FLS, 32, tag, lane, 3);
  STAMP(9);
  issue_h(14);
  issue_h(15);
  {
    const int line = min((4 * cc + lw) * 19 + min(lane, 18), 511);
    touch(line * 128 + (line >> 8) * 128, p.ws + OFF_O + (size_t)(2 * m64) * O_TILE, lb);
  }
  vmwait<5>(); bar_only();
  issue_h(16);
  issue_w(17);
  issue_h(17);
  vmwait<12>(); bar_only();
  vmwait<8>(); bar_only();
  vmwait<0>(); bar_only();
  bar_only();
  STAMP(10);
  bar_only();
  {
    LAS const char* lds = (LAS const char*)(size_t)lb;
    const int row = 8 * wave + (lane >> 3);
    u4v v = *(LAS const u4v*)(lds + L_V + row * 128 + (((lane & 7) ^ ((row >> 1) & 7)) << 4));
    *(GAS u4v*)((char*)p.out + (size_t)(64 * m64 + row) * XROW + cc * 128 + (lane & 7) * 16) = v;
  }
  if (blockIdx.x == 0 && lw == 0 && lane == 0)
    __hip_atomic_store((GAS u32*)(p.ws + WS_EPOCH), E + 1, __ATOMIC_RELAXED, __HIP_MEMORY_SCOPE_AGENT);
  STAMP(11);
}

DI void exp_pair(const f16v& a, float c, f16v& ep, f16v& en) {
  float mx = fmx(a[0], a[1]), mn = fmn(a[0], a[1]);
#pragma unroll
  for (int r = 2; r < 16; r += 2) {
    mx = fmx(fmx(mx, a[r]), a[r + 1]);
    mn = fmn(fmn(mn, a[r]), a[r + 1]);
  }
  mx = xhalf_max(mx);
  mn = xhalf_min(mn);
  const f2v c2 = {c, c}, bp = {-mx * c, -mx * c}, bn = {mn * c, mn * c};
#pragma unroll
  for (int r = 0; r < 16; r += 2) {
    const f2v tp = __builtin_elementwise_fma(pair(a, r), c2, bp), tn = __builtin_elementwise_fma(pair(a, r), -c2, bn);
    ep[r] = __builtin_amdgcn_exp2f(tp[0]); ep[r + 1] = __builtin_amdgcn_exp2f(tp[1]);
    en[r] = __builtin_amdgcn_exp2f(tn[0]); en[r + 1] = __builtin_amdgcn_exp2f(tn[1]);
  }
}
DI float sum28(const f16v& e, int hh) {
  const f2v s0 = (pair(e, 0) + pair(e, 2)) + (pair(e, 4) + pair(e, 6)), s1 = pair(e, 8) + pair(e, 10), s2 = pair(e, 12) + pair(e, 14);
  const f2v t = s0 + s1;
  return xhalf_sum((t[0] + t[1]) + (hh ? 0.f : s2[0] + s2[1]));
}

DI void fold_load(const Params& p, u32 lb, int i, int head, int part, int lane, u4v (&a)[2]) {
  const int r16 = lane & 15, dg = lane >> 4;
  const int col = part == 0 ? XROW : (part == 1 ? 2 * XROW : 0);
  const char* src = (const char*)p.wqkv + (size_t)(32 * i + r16) * QROW + col + head * 56 + (dg == 3 ? 40 : dg * 16);
  a[0] = *(GAS const u4v*)src;
  a[1] = *(GAS const u4v*)(src + 16 * QROW);
  const char* wp = (const char*)p.wphi + head * 1568;
  const u32 dst = lb + L_PHI + part * 2048;
  dma(lane * 16, wp, dst);
  dma(min(lane, 33) * 16, wp + 1024, dst + 1024);
}
DI void fold_run(rsrc_t rs, LAS const char* lds, int i, int head, int part, int lane, const u4v (&a)[2]) {
  const int dg = lane >> 4, r16 = lane & 15;
  asm volatile("s_waitcnt vmcnt(0)" ::: "memory");
  u4v bw[2];
  {
    const int q4 = r16 >> 2, pp = lane & 3;
    LAS const char* b = lds + L_PHI + part * 2048 + (8 * dg + q4) * 56 + pp * 8;
#pragma unroll
    for (int ft = 0; ft < 2; ++ft) {
      u4v v = __builtin_bit_cast(u4v, lds_tr2(b + ft * 32, b + ft * 32 + 4 * 56));
      if (dg == 3) { v[2] = 0u; v[3] = 0u; }
      if (part == 1) {
        const int t = 16 * ft + r16 - 8 * dg;
        const u32 one = (16 * ft + r16 < 28) ? ((t & 1) ? 0x3f800000u : 0x3f80u) : 0u;
#pragma unroll
        for (int q = 0; q < 4; ++q) v[q] = (t >> 1) == q ? one : 0u;
      }
      bw[ft] = v;
    }
  }
  const u32 base = OFF_WP + head * WP_HEAD + (part == 2 ? WP_Q + 2 * i * 1024 : 2 * i * 2048 + part * 1024);
  const u32 step = part == 2 ? 1024 : 2048;
  float cs = 0.f;
#pragma unroll
  for (int t = 0; t < 2; ++t) {
    u4v av = a[t];
    if (dg == 3) av = u4v{av[2], av[3], 0u, 0u};
    const f4v z = {0.f, 0.f, 0.f, 0.f};
    f4v d0 = mfma16(as_bf8(av), as_bf8(bw[0]), z), d1 = mfma16(as_bf8(av), as_bf8(bw[1]), z);
    if (part != 1) {
#pragma unroll
      for (int r = 0; r < 4; ++r) {
        const int v = __float_as_int(d1[r]);
        d1[r] = __int_as_float(__builtin_amdgcn_update_dpp(v, v, 0x15B, 0xf, 0x8, false));
      }
    }
    auto s0 = __builtin_amdgcn_permlane16_swap(pk2(d0[0], d0[1]), pk2(d1[0], d1[1]), false, false);
    auto s1 = __builtin_amdgcn_permlane16_swap(pk2(d0[2], d0[3]), pk2(d1[2], d1[3]), false, false);
    const u4v o = {s0[0], s1[0], s0[1], s1[1]};
    st128(rs, lane * 16, base + t * step, o);
    cs += ((bf_lo(o[0]) + bf_hi(o[0])) + (bf_lo(o[1]) + bf_hi(o[1]))) + ((bf_lo(o[2]) + bf_hi(o[2])) + (bf_lo(o[3]) + bf_hi(o[3])));
  }
  cs = xhalf_sum(cs);
  if (lane < 32) st32(rs, lane * 4, OFF_CSP + ((head * 3 + part) * 16 + i) * 128, __float_as_uint(cs));
}

constexpr int FD = 6, FNB = FD + 1;
template <int U>
DI void f_request(LAS const char* lds, u32 (&xa)[4], u32 (&wa)[4], bf8 (&x)[7][4], bf8 (&wf)[FNB]) {
  if constexpr (U < 112) {
    constexpr int c = U >> 4, s = (U >> 2) & 3, n = U & 3;
    if constexpr ((U & 31) == 0 && U > 0) {
#pragma unroll
      for (int q = 0; q < 4; ++q) {
        xa[q] ^= 65536u;
        wa[q] ^= 65536u;
        asm volatile("" : "+v"(xa[q]), "+v"(wa[q]));
      }
    }
    constexpr u32 xs = fslot(c) & 65535u, ws = xs + SLOT;
    if constexpr (n == 0) x[c][s] = lds_frag(lds + xs + xa[s]);
    wf[U % FNB] = lds_tr2(lds + ws + wa[n] + 4096 * s, lds + ws + wa[n] + 4096 * s + 1024);
  }
}
template <int C, int V>
DI void f_unit(LAS const char* lds, u32 (&xa)[4], u32 (&wa)[4], bf8 (&x)[7][4], bf8 (&wf)[FNB], f16v (&acc)[4]) {
  constexpr int U = 16 * C + V;
  if constexpr (V == 16 - FD && C < 6) bar_only();
  f_request<U + FD>(lds, xa, wa, x, wf);
  acc[V & 3] = mfma32(wf[U % FNB], x[C][V >> 2], acc[V & 3]);
  SCHED();
  if constexpr (V < 15) f_unit<C, V + 1>(lds, xa, wa, x, wf, acc);
}
template <int C>
DI void tick_f(LAS const char* lds, u32 (&xa)[4], u32 (&wa)[4], bf8 (&x)[7][4], bf8 (&wf)[FNB], f16v (&acc)[4]) {
  f_unit<C, 0>(lds, xa, wa, x, wf, acc);
}
template <int U>
DI void f_prime(LAS const char* lds, u32 (&xa)[4], u32 (&wa)[4], bf8 (&x)[7][4], bf8 (&wf)[FNB]) {
  f_request<U>(lds, xa, wa, x, wf);
  if constexpr (U + 1 < FD) f_prime<U + 1>(lds, xa, wa, x, wf);
}

DI void compute_main(const Params& p, LAS char* lds, const Role& R, int w, int lane, int wave) {
  const rsrc_t rs = __builtin_amdgcn_make_buffer_rsrc(p.ws, 0, WS_USED, 0x00020000);
  const int i = R.i, j = R.j;
  const int hh = lane >> 5, l31 = lane & 31;
  const bool ffn = j < 14;
  const bool fold = i < 14 && w < 3;
  const u32 E = *(CAS const u32*)(p.ws + WS_EPOCH);
  const u64 tag = tag64(E + 1);
  u32 xs7[7];
  const int stok = 128 * i + 8 * j + 2 * w + hh;
#pragma unroll
  for (int m = 0; m < 7; ++m) xs7[m] = *(GAS const u32*)((const char*)p.x + (size_t)stok * XROW + m * 128 + l31 * 4);
  auto stats = [&]() {
    f2v s2 = {0.f, 0.f};
#pragma unroll
    for (int m = 0; m < 7; ++m) s2 += f2v{bf_lo(xs7[m]), bf_hi(xs7[m])};
    const float mean = half_sum(s2[0] + s2[1]) * (1.f / D_);
    f2v q2 = {0.f, 0.f};
#pragma unroll
    for (int m = 0; m < 7; ++m) {
      const f2v a = f2v{bf_lo(xs7[m]), bf_hi(xs7[m])} - mean;
      q2 = __builtin_elementwise_fma(a, a, q2);
    }
    const float sigma = __builtin_amdgcn_sqrtf(half_sum(q2[0] + q2[1]) * (1.f / D_) + 1e-5f);
    if (l31 == 0) st128(rs, stok * 16, OFF_ST, u4v{__float_as_uint(mean), __float_as_uint(sigma), (u32)tag, (u32)(tag >> 32)});
  };
  if (fold) {
    u4v fa[2];
    fold_load(p, (u32)(size_t)lds, i, j, w, lane, fa);
    SCHED();
    bar_only();
    SCHED();
    STAMP(22);
    fold_run(rs, lds, i, j, w, lane, fa);
    stats();
  } else if (w < 3) {
    bar_only();
    stats();
  } else {
    u4v fc[7];
    const char* src = (const char*)p.w1 + (size_t)(28 * i + (lane >> 4)) * W1ROW + R.jw * 256 + (lane & 15) * 16;
#pragma unroll
    for (int q = 0; q < 7; ++q) fc[q] = *(GAS const u4v*)(src + (size_t)q * 4 * W1ROW);
    SCHED();
    bar_only();
    SCHED();
    float cs[8];
#pragma unroll
    for (int e = 0; e < 8; ++e) cs[e] = 0.f;
#pragma unroll
    for (int q = 0; q < 7; ++q)
#pragma unroll
      for (int e = 0; e < 4; ++e) { cs[2 * e] += bf_lo(fc[q][e]); cs[2 * e + 1] += bf_hi(fc[q][e]); }
#pragma unroll
    for (int e = 0; e < 8; ++e) {
      auto r = __builtin_amdgcn_permlane16_swap(__float_as_uint(cs[e]), __float_as_uint(cs[e]), false, false);
      cs[e] = xhalf_sum(__uint_as_float(r[0]) + __uint_as_float(r[1]));
    }
    if (ffn && lane < 32) {
      const int o = (lane >> 4) * 4;
      st128(rs, (lane & 15) * 32 + (lane >> 4) * 16, OFF_C1P + (j * 16 + i) * 512,
            u4v{__float_as_uint(o ? cs[4] : cs[0]), __float_as_uint(o ? cs[5] : cs[1]), __float_as_uint(o ? cs[6] : cs[2]), __float_as_uint(o ? cs[7] : cs[3])});
    }
    stats();
  }
  STAMP(23);
  u32 xa[4], wa[4];
  {
    const int row = 32 * w + l31;
    const u32 b = row * 128 + ((hh ^ ((row >> 1) & 7)) << 4);
#pragma unroll
    for (int s = 0; s < 4; ++s) xa[s] = b ^ (32 * s);
    const int q4 = (lane & 15) >> 2, pp = lane & 3, blk = (lane >> 4) & 1;
#pragma unroll
    for (int n = 0; n < 4; ++n) {
      const int nt = (n + w) & 3;
      wa[n] = (8 * hh + q4) * 256 + (4 * (nt ^ q4) + 2 * blk + (pp >> 1)) * 16 + 8 * (pp & 1);
    }
  }
  bf8 x[7][4], wf[FNB];
  f16v acc[4];
#pragma unroll
  for (int n = 0; n < 4; ++n)
#pragma unroll
    for (int r = 0; r < 16; ++r) acc[n][r] = 0.f;

  if (w == 0 && lane < 4) *(LAS volatile u32*)(lds + L_CNT + lane * 4) = 0u;
  bar();
  STAMP(2);
  f_prime<0>(lds, xa, wa, x, wf);
  tick_f<0>(lds, xa, wa, x, wf, acc);
  STAMP(30);
  vmwait<1>();
  if (lane == 0) {
    if (fold) __hip_atomic_store((GAS u64*)(p.ws + FL_P0 + ((j * 3 + w) * 16 + i) * FLS), tag, __ATOMIC_RELAXED, __HIP_MEMORY_SCOPE_AGENT);
    if (ffn && w == 3) __hip_atomic_store((GAS u64*)(p.ws + FL_C1 + (j * 16 + i) * FLS), tag, __ATOMIC_RELAXED, __HIP_MEMORY_SCOPE_AGENT);
  }
  tick_f<1>(lds, xa, wa, x, wf, acc);
  STAMP(31);
  const int token = 128 * i + 32 * w + l31;
  u4v sg = ld128(rs, token * 16, OFF_ST);
  tick_f<2>(lds, xa, wa, x, wf, acc);
  STAMP(32);
  tick_f<3>(lds, xa, wa, x, wf, acc);
  STAMP(33);
  float mean = __uint_as_float(sg[0]), sigma = __uint_as_float(sg[1]);
  if (!__all(sg[2] == (u32)tag && sg[3] == (u32)(tag >> 32))) stat_wait(p.ws, rs, token, tag, lane, mean, sigma);
  tick_f<4>(lds, xa, wa, x, wf, acc);
  STAMP(34);
  bf8 xaug;
  {
    u32 mh, ml, sh, sl;
    split2(-mean, mh, ml);
    split2(sigma, sh, sl);
    u4v r = {mh | (ml << 16), mh | (sh << 16), sl, 0u};
    if (hh) r = u4v{0u, 0u, 0u, 0u};
    xaug = as_bf8(r);
  }
  const float rstd = __builtin_amdgcn_rcpf(sigma);
  tick_f<5>(lds, xa, wa, x, wf, acc);
  STAMP(35);
  tick_f<6>(lds, xa, wa, x, wf, acc);
  STAMP(4);
  bar();
  STAMP(6);
  const u32 vl = lane * 16;
  if (ffn) {
#pragma unroll
    for (int n = 0; n < 4; ++n) acc[n] = mfma32(lds_frag(lds + L_AUG + ((n + w) & 3) * 1024 + vl), xaug, acc[n]);
    LAS char* hb = lds + L_HS + w * 8192 + vl;
#pragma unroll
    for (int n = 0; n < 4; ++n) {
#pragma unroll
      for (int r = 0; r < 16; r += 2) {
        const f2v t = {fmx(acc[n][r], 0.f), fmx(acc[n][r + 1], 0.f)};
        const f2v u = t * t;
        acc[n][r] = u[0];
        acc[n][r + 1] = u[1];
      }
#pragma unroll
      for (int s = 0; s < 2; ++s) *(LAS bf8*)(hb + (2 * ((n + w) & 3) + s) * 1024) = frag8(acc[n], s);
    }
  }
  bar();
  STAMP(5);
  f16v ak, av;
#pragma unroll
  for (int r = 0; r < 16; ++r) { ak[r] = 0.f; av[r] = 0.f; }
  {
    LAS const char* wb = lds + vl;
    auto kvo = [](int ks) -> u32 { return ks < 16 ? L_KVA + ks * 2048 : L_KVB + (ks - 16) * 2048; };
    auto kvo2 = [&](int ks) -> u32 { return ks < 28 ? kvo(ks) : L_AUG + 4 * 1024; };
    bf8 fk[4], fv[4];
#pragma unroll
    for (int ks = 0; ks < 3; ++ks) {
      fk[ks] = lds_frag(wb + kvo(ks));
      fv[ks] = lds_frag(wb + kvo(ks) + 1024);
    }
#pragma unroll
    for (int ks = 0; ks < 29; ++ks) {
      if (ks + 3 < 29) {
        fk[(ks + 3) & 3] = lds_frag(wb + kvo2(ks + 3));
        fv[(ks + 3) & 3] = lds_frag(wb + kvo2(ks + 3) + 1024);
      }
      const bf8 xb = ks < 28 ? x[(ks < 28 ? ks : 0) >> 2][ks & 3] : xaug;
      ak = mfma32(fk[ks & 3], xb, ak);
      av = mfma32(fv[ks & 3], xb, av);
      __builtin_amdgcn_sched_group_barrier(0x8, 1, 0);
      __builtin_amdgcn_sched_group_barrier(0x100, 1, 0);
      __builtin_amdgcn_sched_group_barrier(0x8, 1, 0);
      __builtin_amdgcn_sched_group_barrier(0x100, 1, 0);
      SCHED();
    }
  }
  cnt_arrive((u32)(size_t)lds + L_CNT + 4, lane);
  STAMP(7);
  const float cexp = rstd * LOG2E;
  {
    f16v ep, en;
    exp_pair(ak, cexp, ep, en);
    const float ap = rstd * __builtin_amdgcn_rcpf(sum28(ep, hh)), an = rstd * __builtin_amdgcn_rcpf(sum28(en, hh));
    const int row = 32 * w + l31;
    LAS char* base = lds + row * 64 + 8 * hh;
    const int key = (l31 >> 1) & 3;
#pragma unroll
    for (int q = 0; q < 4; ++q) {
      const u32 o = (q ^ key) << 4;
      *(LAS u2v*)(base + L_PHI + o) = u2v{pk2v(pair(ep, 4 * q) * ap), pk2v(pair(ep, 4 * q + 2) * ap)};
      *(LAS u2v*)(base + L_PHI + 8192 + o) = u2v{pk2v(pair(en, 4 * q) * an), pk2v(pair(en, 4 * q + 2) * an)};
      *(LAS u2v*)(base + L_V + o) = u2v{pk2(av[4 * q], av[4 * q + 1]), pk2(av[4 * q + 2], av[4 * q + 3])};
    }
  }
  STAMP(8);
  cnt_arrive((u32)(size_t)lds + L_CNT, lane);
  if (w < 2) {
    cnt_wait((u32)(size_t)lds + L_CNT);
    STAMP(9);
    const int q4 = (lane & 15) >> 2, pp = lane & 3, blk = (lane >> 4) & 1;
    u32 ta[2];
#pragma unroll
    for (int t = 0; t < 2; ++t) {
      const int row = 8 * hh + 4 * t + q4;
      ta[t] = row * 64 + (((2 * blk + (pp >> 1)) ^ ((row >> 1) & 3)) << 4) + 8 * (pp & 1);
    }
    f16v kv;
#pragma unroll
    for (int r = 0; r < 16; ++r) kv[r] = 0.f;
    LAS const char* pa = lds + L_PHI + w * 8192;
    LAS const char* pb = lds + L_V;
#pragma unroll
    for (int ks = 0; ks < 8; ++ks)
      kv = mfma32(lds_tr2(pa + ta[0] + ks * 1024, pa + ta[1] + ks * 1024), lds_tr2(pb + ta[0] + ks * 1024, pb + ta[1] + ks * 1024), kv);
    const u32 o = OFF_KVP + (j * 2 + w) * KVP_HS + i * 2048;
    st128(rs, vl, o, __builtin_bit_cast(u4v, frag8(kv, 0)));
    st128(rs, vl, o + 1024, __builtin_bit_cast(u4v, frag8(kv, 1)));
  }
  STAMP(18);
  cnt_wait((u32)(size_t)lds + L_CNT + 8);
  f16v aq;
#pragma unroll
  for (int r = 0; r < 16; ++r) aq[r] = 0.f;
  {
    LAS const char* wb = lds + L_Q + vl;
    bf8 fq[6];
#pragma unroll
    for (int ks = 0; ks < 5; ++ks) fq[ks] = lds_frag(wb + ks * 1024);
#pragma unroll
    for (int ks = 0; ks < 29; ++ks) {
      if (ks + 5 < 28) fq[(ks + 5) % 6] = lds_frag(wb + (ks + 5) * 1024);
      else if (ks + 5 == 28) fq[(ks + 5) % 6] = lds_frag(lds + L_AUG + 6 * 1024 + vl);
      aq = mfma32(fq[ks % 6], ks < 28 ? x[(ks < 28 ? ks : 0) >> 2][ks & 3] : xaug, aq);
      SCHED();
      if (ks == 15 && w < 2) flag_set(p.ws, FL_KV + ((j * 2 + w) * 16 + i) * FLS, tag, lane);
    }
  }
  STAMP(10);
  bf8 eq[2][2];
  auto qepi = [&]() {
    f16v ep, en;
    exp_pair(aq, cexp, ep, en);
    eq[0][0] = frag8(ep, 0); eq[0][1] = frag8(ep, 1);
    eq[1][0] = frag8(en, 0); eq[1][1] = frag8(en, 1);
  };
  const int m64 = R.m64, cc = R.cc;
  const int bm = w & 1, bn = w >> 1;
  u32 ha, wb;
  {
    const int q4 = (lane & 15) >> 2, pp = lane & 3, blk = (lane >> 4) & 1;
    ha = bm * 8192 + bn * 4096 + vl;
    wb = bn * 8192 + (4 * hh + q4) * 128 + (4 * (q4 >> 1) + 2 * blk + (pp >> 1)) * 16 + 8 * (pp & 1);
  }
  const u4v sg2 = ld128(rs, (64 * m64 + 32 * bm + l31) * 16, OFF_ST);
  const u32 b2 = *((const GAS u16*)p.b2 + cc * 64 + bn * 32 + l31);
  f16v g0, g1, a0, a1;
#pragma unroll
  for (int r = 0; r < 16; ++r) { a0[r] = 0.f; a1[r] = 0.f; }
  bf8 fh[4], f0[4], f1[4];
  u32 hp = 0, wp = 0;
  auto point = [&](int g) { hp = hslot(g) + ha; wp = (g == 16 ? L_PHI : wslot(g)) + wb; };
  auto req = [&](int ks) {
    fh[ks] = lds_frag(lds + hp + ks * 1024);
    f0[ks] = lds_tr2(lds + wp + ks * 2048, lds + wp + ks * 2048 + 1024);
    f1[ks] = lds_tr2(lds + (wp ^ 64) + ks * 2048, lds + (wp ^ 64) + ks * 2048 + 1024);
  };
  auto mm = [&](int ks) {
    a0 = mfma32(f0[ks], fh[ks], a0);
    a1 = mfma32(f1[ks], fh[ks], a1);
    __builtin_amdgcn_sched_group_barrier(0x8, 1, 0);
    __builtin_amdgcn_sched_group_barrier(0x100, 3, 0);
    __builtin_amdgcn_sched_group_barrier(0x8, 1, 0);
    __builtin_amdgcn_sched_group_barrier(0x100, 2, 0);
    SCHED();
  };
  auto tick = [&](int g) {
    req(3); mm(0);
    bar_only();
    point(g + 1);
    req(0); mm(1);
    req(1); mm(2);
    req(2); mm(3);
  };
  const u32 krflag = FL_KR + (j * 8 + min(lane, 7)) * FLS;
  int g = 0;
  u4v kt[4];
  auto oload = [&]() {
#pragma unroll
    for (int q = 0; q < 4; ++q) kt[q] = ld128(rs, vl, OFF_KVT + (j * 4 + q) * 1024);
  };
  auto ostore = [&]() {
    f16v np, nn;
#pragma unroll
    for (int r = 0; r < 16; ++r) { np[r] = 0.f; nn[r] = 0.f; }
    np = mfma32(as_bf8(kt[0]), eq[0][0], np);
    nn = mfma32(as_bf8(kt[2]), eq[1][0], nn);
    np = mfma32(as_bf8(kt[1]), eq[0][1], np);
    nn = mfma32(as_bf8(kt[3]), eq[1][1], nn);
    const float ia = __builtin_amdgcn_rcpf(xhalf_hi(np[13])), ib = __builtin_amdgcn_rcpf(xhalf_hi(nn[13]));
    const float den = xhalf_hi(np[12]) * ia + xhalf_hi(nn[12]) * ib + 1e-6f;
    const float id = __builtin_amdgcn_rcpf(den), fa = ia * id, fb = ib * id;
    u32 ov[8];
#pragma unroll
    for (int r = 0; r < 16; r += 2) ov[r >> 1] = pk2v(pair(np, r) * fa + pair(nn, r) * fb);
    if (hh) { ov[6] = 0u; ov[7] = 0u; }
    const u32 ob = OFF_O + (4 * i + w) * O_TILE + 2 * j * 1024;
    st128(rs, vl, ob, u4v{ov[0], ov[1], ov[2], ov[3]});
    st128(rs, vl, ob + 1024, u4v{ov[4], ov[5], ov[6], ov[7]});
  };
  const u32 oflag = FL_O + ((i * 16 + j) * 4 + w) * FLS;
  if (R.bwork) {
    bar();
    point(0);
    req(0); req(1); req(2);
    SCHED();
    tick(0);
    tick(1);
    g = 2;
    qepi();
    SCHED();
    u64 kr = 0;
    bool ready = false;
#pragma unroll 1
    while (g < 10) {
      tick(g);
      ++g;
      if (__all(kr == tag)) { ready = true; break; }
      if (g >= 4) kr = __hip_atomic_load((GAS u64*)(p.ws + krflag), __ATOMIC_RELAXED, __HIP_MEMORY_SCOPE_AGENT);
    }
    if (!ready) flag_wait(p.ws, krflag, 8, tag, lane, 6);
    STAMP(14);
    oload();
    tick(g); ++g;
    ostore();
    tick(g); ++g;
    flag_set(p.ws, oflag, tag, lane);
    STAMP(15);
  } else {
    const int hs = R.rho, sk = w & 1, hf = w >> 1;
    flag_wait(p.ws, FL_KV + (hs * 16 + lane) * FLS, 16, tag, lane, 5);
    STAMP(12);
    const u32 o = OFF_KVP + hs * KVP_HS + sk * 1024 + hf * 512;
    u4v t[8];
#pragma unroll
    for (int q = 0; q < 8; ++q) t[q] = ld128(rs, hh * 8 * 2048 + l31 * 16, o + q * 2048);
    f2v sm[4];
#pragma unroll
    for (int e = 0; e < 4; ++e) sm[e] = f2v{bf_lo(t[0][e]), bf_hi(t[0][e])};
#pragma unroll
    for (int q = 1; q < 8; ++q)
#pragma unroll
      for (int e = 0; e < 4; ++e) sm[e] += f2v{bf_lo(t[q][e]), bf_hi(t[q][e])};
    u4v r;
#pragma unroll
    for (int e = 0; e < 4; ++e) r[e] = pk2(xhalf_sum(sm[e][0]), xhalf_sum(sm[e][1]));
    if (l31 == 29) r = u4v{0x3f803f80u, 0x3f803f80u, 0x3f803f80u, 0x3f803f80u};
    if (l31 >= 30) r = u4v{0u, 0u, 0u, 0u};
    if (sk == 1 && hf == 1) { r[2] = 0u; r[3] = 0u; }
    if (lane < 32) st128(rs, l31 * 16, OFF_KVT + (hs * 2 + sk) * 1024 + hf * 512, r);
    flag_set(p.ws, FL_KR + ((hs * 2 + sk) * 2 + hf) * FLS, tag, lane);
    STAMP(13);
    qepi();
    flag_wait(p.ws, krflag, 8, tag, lane, 6);
    STAMP(14);
    oload();
    ostore();
    flag_set(p.ws, oflag, tag, lane);
    STAMP(15);
    return;
  }
#pragma unroll 1
  for (; g < 13; ++g) tick(g);
  req(3); mm(0); mm(1); mm(2); mm(3);
  STAMP(24);
  g0 = a0; g1 = a1;
#pragma unroll
  for (int r = 0; r < 16; ++r) { a0[r] = 0.f; a1[r] = 0.f; }
  bar();
  point(14);
  req(0); req(1); req(2);
  SCHED();
#pragma unroll 1
  for (g = 14; g < 17; ++g) tick(g);
  req(3); mm(0); mm(1); mm(2); mm(3);
  f16v ag, accb;
  {
    LAS char* dst = lds + (bm * 2 + (1 - bn)) * 8192 + vl;
    LAS const char* src = lds + (bm * 2 + bn) * 8192 + vl;
#pragma unroll
    for (int q = 0; q < 4; ++q) {
      const f16v& gs = bn ? g0 : g1;
      const f16v& as = bn ? a0 : a1;
      *(LAS f4v*)(dst + q * 1024) = f4v{gs[4 * q], gs[4 * q + 1], gs[4 * q + 2], gs[4 * q + 3]};
      *(LAS f4v*)(dst + 4096 + q * 1024) = f4v{as[4 * q], as[4 * q + 1], as[4 * q + 2], as[4 * q + 3]};
    }
    bar();
#pragma unroll
    for (int q = 0; q < 4; ++q) {
      const f4v gv = *(LAS const f4v*)(src + q * 1024), sv = *(LAS const f4v*)(src + 4096 + q * 1024);
#pragma unroll
      for (int e = 0; e < 4; ++e) {
        ag[4 * q + e] = (bn ? g1[4 * q + e] : g0[4 * q + e]) + gv[e];
        accb[4 * q + e] = (bn ? a1[4 * q + e] : a0[4 * q + e]) + sv[e];
      }
    }
  }
  STAMP(16);
  {
    float m2 = __uint_as_float(sg2[0]), s2 = __uint_as_float(sg2[1]);
    if (!__all(sg2[2] == (u32)tag && sg2[3] == (u32)(tag >> 32))) stat_wait(p.ws, rs, 64 * m64 + 32 * bm + l31, tag, lane, m2, s2);
    const float var = s2 * s2;
    u32 vh, vlo;
    split2(var, vh, vlo);
    u4v xa2 = {vh | (vlo << 16), 0u, 0u, 0u};
    u4v wa3 = {b2 | (b2 << 16), 0u, 0u, 0u};
    if (hh) { xa2[0] = 0u; wa3[0] = 0u; }
    ag = mfma32(as_bf8(wa3), as_bf8(xa2), ag);
    const float r2 = __builtin_amdgcn_rcpf(var);
    const int row = 32 * bm + l31;
    LAS char* base = lds + L_V + row * 128 + 8 * hh;
    const int key = (row >> 1) & 7;
#pragma unroll
    for (int q = 0; q < 4; ++q) {
      LAS u2v* ptr = (LAS u2v*)(base + (((4 * bn + q) ^ key) << 4));
      const u2v xv = *ptr;
      float o[4];
#pragma unroll
      for (int e = 0; e < 4; ++e) {
        const float gp = ag[4 * q + e] * r2;
        const float sg = gp * __builtin_amdgcn_rcpf(1.f + __builtin_amdgcn_exp2f(-gp * LOG2E));
        const float xr = (e & 1) ? bf_hi(xv[e >> 1]) : bf_lo(xv[e >> 1]);
        o[e] = __builtin_fmaf(accb[4 * q + e], sg, xr);
      }
      *ptr = u2v{pk2(o[0], o[1]), pk2(o[2], o[3])};
    }
  }
  bar();
  {
    const int row = 8 * wave + (lane >> 3);
    u4v v = *(LAS const u4v*)(lds + L_V + row * 128 + (((lane & 7) ^ ((row >> 1) & 7)) << 4));
    *(GAS u4v*)((char*)p.out + (size_t)(64 * m64 + row) * XROW + cc * 128 + (lane & 7) * 16) = v;
  }
  STAMP(17);
  DIAG_WAIT();
  STAMPC(63);
}

__global__ __launch_bounds__(512, 2) void fused_kernel(Params p) {
  extern __shared__ __attribute__((aligned(16))) char smem[];
  LAS char* lds = (LAS char*)smem;
  const int lane = threadIdx.x & 63;
  const int wave = __builtin_amdgcn_readfirstlane(threadIdx.x >> 6);
  const int b = blockIdx.x, chi = b & 7, sidx = b >> 3;
  Role R;
  R.i = 8 * (chi & 1) + (sidx & 7);
  R.j = 4 * (chi >> 1) + (sidx >> 3);
  R.jw = min(R.j, 13);
  R.bwork = sidx < 28;
  R.tq = R.bwork ? sidx / 7 : 0;
  R.cc = R.bwork ? sidx % 7 : 0;
  R.m64 = 4 * chi + R.tq;
  R.rho = 4 * chi + (sidx - 28);
  R.sidx = sidx;
  DIAG_INIT();
  if (wave == 0) *(LAS volatile u32*)(size_t)(L_DIAG + 61 * 4) = 0x100u | ((u32)__builtin_amdgcn_s_getreg((3 << 11) | 20) & 0xFu);
  STAMP(0);
  DIAG_WAIT();
  STAMPC(62);
  DIAG_WAIT();
  if (wave >= 4) loader_main(p, (u32)(size_t)lds, R, wave - 4, lane, wave);
  else compute_main(p, lds, R, wave, lane, wave);
  DIAG_DUMP();
  if ((wave & 3) == 0 && lane == 0 && (blockIdx.x % 37) == 0) {
    LAS volatile u32* st = (LAS volatile u32*)(size_t)(L_DIAG + (wave >> 2) * 256);
    const u32 t0 = st[0];
#define D_(k) (st[k] ? (int)(st[k] - t0) : -1)
    if (wave == 0) printf("B%d C t0=%u | 22:%d 23:%d 2:%d 30:%d 31:%d 32:%d 33:%d 34:%d 35:%d 4:%d 6:%d 5:%d 7:%d 8:%d 9:%d 18:%d 10:%d 14:%d 15:%d 24:%d 16:%d 17:%d\n", (int)blockIdx.x, t0,
      D_(22), D_(23), D_(2), D_(30), D_(31), D_(32), D_(33), D_(34), D_(35), D_(4), D_(6), D_(5), D_(7), D_(8), D_(9), D_(18), D_(10), D_(14), D_(15), D_(24), D_(16), D_(17));
    else printf("B%d L t0=%u | 12:%d 13:%d 1:%d 2:%d 3:%d 4:%d 5:%d 14:%d 6:%d 7:%d 8:%d 9:%d 10:%d 11:%d\n", (int)blockIdx.x, t0,
      D_(12), D_(13), D_(1), D_(2), D_(3), D_(4), D_(5), D_(14), D_(6), D_(7), D_(8), D_(9), D_(10), D_(11));
  }
}

extern "C" void kernel_launch(void* const* d_in, const int* in_sizes, int n_in, void* d_out, int out_size, void* d_ws,
                              size_t ws_size, hipStream_t stream) {
  Params p;
  p.x = (const u16*)d_in[0];
  p.wqkv = (const u16*)d_in[1];
  p.wphi = (const u16*)d_in[2];
  p.wo = (const u16*)d_in[3];
  p.w1 = (const u16*)d_in[5];
  p.b1 = (const u16*)d_in[6];
  p.w2 = (const u16*)d_in[7];
  p.b2 = (const u16*)d_in[8];
  p.out = (u16*)d_out;
  p.ws = (char*)d_ws;
  (void)hipFuncSetAttribute((const void*)fused_kernel, hipFuncAttributeMaxDynamicSharedMemorySize, LDS_BYTES);
  fused_kernel<<<256, 512, LDS_BYTES, stream>>>(p);
}
```

```cpp
#define TOUCH_POS 1
#include <hip/hip_runtime.h>
#include <hip/hip_bf16.h>
#include <stdint.h>

typedef unsigned short u16;
typedef uint32_t u32;
typedef uint64_t u64;
typedef __bf16 bf2 __attribute__((ext_vector_type(2)));
typedef __bf16 bf4 __attribute__((ext_vector_type(4)));
typedef __bf16 bf8 __attribute__((ext_vector_type(8)));
typedef float f4v __attribute__((ext_vector_type(4)));
typedef float f16v __attribute__((ext_vector_type(16)));
typedef u32 u2v __attribute__((ext_vector_type(2)));
typedef u32 u4v __attribute__((ext_vector_type(4)));
#define LAS __attribute__((address_space(3)))
#define GAS __attribute__((address_space(1)))
#define CAS __attribute__((address_space(4)))
#define DI __device__ __forceinline__

constexpr int S_ = 2048, D_ = 448, NH = 16, DH = 28, FF = 1792;
constexpr int XROW = D_ * 2;
constexpr int QROW = 3 * D_ * 2;
constexpr int W1ROW = FF * 2;

constexpr u32 WS_EPOCH = 0, WS_ERR = 128;
constexpr u32 FL0 = 4096, FLS = 128;
constexpr u32 FL_P0 = FL0;
constexpr u32 FL_C1 = FL_P0 + 768 * FLS;
constexpr u32 FL_H = FL_C1 + 1024 * FLS;
constexpr u32 FL_KV = FL_H + 1024 * FLS;
constexpr u32 FL_KR = FL_KV + 512 * FLS;
constexpr u32 FL_O = FL_KR + 128 * FLS;
constexpr u32 OFF_ST = FL_O + 1024 * FLS;
constexpr u32 OFF_WP = OFF_ST + S_ * 16;
constexpr u32 WP_HEAD = 84 * 1024, WP_Q = 56 * 1024;
constexpr u32 OFF_CSP = OFF_WP + NH * WP_HEAD;
constexpr u32 OFF_C1P = OFF_CSP + NH * 3 * 2048;
constexpr u32 H_TILE = 112 * 1024 + 128, KVP_HS = 16 * 2048 + 128, O_TILE = 32 * 1024 + 128;
constexpr u32 OFF_H = OFF_C1P + 256 * 512;
constexpr u32 OFF_KVP = OFF_H + 64 * H_TILE;
constexpr u32 OFF_KVT = OFF_KVP + NH * 2 * KVP_HS;
constexpr u32 OFF_O = OFF_KVT + NH * 4 * 1024;
constexpr u32 OFF_DIAG = OFF_O + 64 * O_TILE;
constexpr u32 WS_USED = OFF_DIAG + 256 * 8 * 64 * 8;

constexpr u32 SLOT = 16384;
constexpr u32 L_PHI = 8 * SLOT;
constexpr u32 L_V = L_PHI + 16384;
constexpr u32 L_AUG = L_V + 8192;
constexpr u32 L_DUMMY = L_AUG + 7168 + 512;
constexpr u32 L_CNT = L_DUMMY + 256;
constexpr u32 LDS_BYTES = 163840;

constexpr float LOG2E = 1.4426950408889634f;
constexpr u32 SPIN_CAP = 1u << 18;

struct Params {
  const u16 *x, *wqkv, *wphi, *wo, *w1, *b1, *w2, *b2;
  u16* out;
  char* ws;
};

#define STAMP(id)
#define STAMPC(id)
#define DIAG_WAIT()
#define DIAG_INIT()
#define DIAG_DUMP()

DI float bf_lo(u32 u) { return __uint_as_float(u << 16); }
DI float bf_hi(u32 u) { return __uint_as_float(u & 0xffff0000u); }
DI u32 pk2(float a, float b) {
  bf2 v = {(__bf16)a, (__bf16)b};
  return __builtin_bit_cast(u32, v);
}
DI u32 bf_rn(float a) { return pk2(a, 0.f) & 0xffffu; }
DI bf8 as_bf8(u4v v) { return __builtin_bit_cast(bf8, v); }
DI bf8 frag8(const f16v& a, int s) {
  u4v r = {pk2(a[8 * s], a[8 * s + 1]), pk2(a[8 * s + 2], a[8 * s + 3]), pk2(a[8 * s + 4], a[8 * s + 5]), pk2(a[8 * s + 6], a[8 * s + 7])};
  return as_bf8(r);
}
DI f16v mfma32(bf8 a, bf8 b, f16v c) { return __builtin_amdgcn_mfma_f32_32x32x16_bf16(a, b, c, 0, 0, 0); }
DI f4v mfma16(bf8 a, bf8 b, f4v c) { return __builtin_amdgcn_mfma_f32_16x16x32_bf16(a, b, c, 0, 0, 0); }
DI bf8 lds_frag(LAS const char* p) { return *(LAS const bf8*)p; }
DI bf8 lds_tr2(LAS const char* p0, LAS const char* p1) {
  bf4 a = __builtin_amdgcn_ds_read_tr16_b64_v4bf16((LAS bf4*)p0);
  bf4 b = __builtin_amdgcn_ds_read_tr16_b64_v4bf16((LAS bf4*)p1);
  return __builtin_shufflevector(a, b, 0, 1, 2, 3, 4, 5, 6, 7);
}
DI float xhalf_max(float v) {
  auto r = __builtin_amdgcn_permlane32_swap(__float_as_uint(v), __float_as_uint(v), false, false);
  return __builtin_elementwise_maximum(__uint_as_float(r[0]), __uint_as_float(r[1]));
}
DI float xhalf_min(float v) {
  auto r = __builtin_amdgcn_permlane32_swap(__float_as_uint(v), __float_as_uint(v), false, false);
  return __builtin_elementwise_minimum(__uint_as_float(r[0]), __uint_as_float(r[1]));
}
DI float xhalf_sum(float v) {
  auto r = __builtin_amdgcn_permlane32_swap(__float_as_uint(v), __float_as_uint(v), false, false);
  return __uint_as_float(r[0]) + __uint_as_float(r[1]);
}
DI float xhalf_hi(float v) {
  auto r = __builtin_amdgcn_permlane32_swap(__float_as_uint(v), __float_as_uint(v), false, false);
  return __uint_as_float(r[1]);
}
DI float dpp_ror(float v, int n) {
  const int iv = __builtin_bit_cast(int, v);
  int r;
  switch (n) {
    case 8: r = __builtin_amdgcn_update_dpp(0, iv, 0x128, 0xf, 0xf, false); break;
    case 4: r = __builtin_amdgcn_update_dpp(0, iv, 0x124, 0xf, 0xf, false); break;
    case 2: r = __builtin_amdgcn_update_dpp(0, iv, 0x122, 0xf, 0xf, false); break;
    default: r = __builtin_amdgcn_update_dpp(0, iv, 0x121, 0xf, 0xf, false); break;
  }
  return __builtin_bit_cast(float, r);
}
DI float half_sum(float v) {
  v += dpp_ror(v, 8); v += dpp_ror(v, 4); v += dpp_ror(v, 2); v += dpp_ror(v, 1);
  auto r = __builtin_amdgcn_permlane16_swap(__float_as_uint(v), __float_as_uint(v), false, false);
  return __uint_as_float(r[0]) + __uint_as_float(r[1]);
}
typedef float f2v __attribute__((ext_vector_type(2)));
DI f2v pair(const f16v& a, int r) { return f2v{a[r], a[r + 1]}; }
DI u32 pk2v(f2v v) { return pk2(v[0], v[1]); }
DI void bar() { asm volatile("s_waitcnt lgkmcnt(0)\n\ts_barrier" ::: "memory"); }
DI void bar_only() { asm volatile("s_barrier" ::: "memory"); }
template <int N> DI void vmwait() { asm volatile("s_waitcnt vmcnt(%0)" ::"i"(N) : "memory"); }
#define SCHED() __builtin_amdgcn_sched_barrier(0)

DI void dma(u32 voff, const void* sbase, u32 ldsaddr) {
  asm volatile("s_mov_b32 m0, %2\n\ts_nop 0\n\tglobal_load_lds_dwordx4 %0, %1" ::"v"(voff), "s"(sbase), "s"(ldsaddr) : "memory");
}
DI void dma_sc1(u32 voff, const void* sbase, u32 ldsaddr) {
  asm volatile("s_mov_b32 m0, %2\n\ts_nop 0\n\tglobal_load_lds_dwordx4 %0, %1 sc1" ::"v"(voff), "s"(sbase), "s"(ldsaddr) : "memory");
}

DI void dma4(u32 v0, u32 v1, u32 v2, u32 v3, const void* sbase, u32 ldsaddr) {
  asm volatile("s_mov_b32 m0, %5\n\ts_nop 0\n\tglobal_load_lds_dwordx4 %0, %4\n\tglobal_load_lds_dwordx4 %1, %4 offset:1024\n\t"
               "global_load_lds_dwordx4 %2, %4 offset:2048\n\tglobal_load_lds_dwordx4 %3, %4 offset:3072"
               ::"v"(v0), "v"(v1), "v"(v2), "v"(v3), "s"(sbase), "s"(ldsaddr) : "memory");
}
template <int N> DI void dma_lin(u32 v, const void* sbase, u32 ldsaddr) {
  if constexpr (N == 1)
    asm volatile("s_mov_b32 m0, %2\n\ts_nop 0\n\tglobal_load_lds_dwordx4 %0, %1 sc1" ::"v"(v), "s"(sbase), "s"(ldsaddr) : "memory");
  else if constexpr (N == 2)
    asm volatile("s_mov_b32 m0, %2\n\ts_nop 0\n\tglobal_load_lds_dwordx4 %0, %1 sc1\n\tglobal_load_lds_dwordx4 %0, %1 offset:1024 sc1"
                 ::"v"(v), "s"(sbase), "s"(ldsaddr) : "memory");
  else
    asm volatile("s_mov_b32 m0, %2\n\ts_nop 0\n\tglobal_load_lds_dwordx4 %0, %1 sc1\n\tglobal_load_lds_dwordx4 %0, %1 offset:1024 sc1\n\t"
                 "global_load_lds_dwordx4 %0, %1 offset:2048 sc1\n\tglobal_load_lds_dwordx4 %0, %1 offset:3072 sc1"
                 ::"v"(v), "s"(sbase), "s"(ldsaddr) : "memory");
}

typedef __amdgpu_buffer_rsrc_t rsrc_t;
DI u4v ld128(rsrc_t r, u32 voff, u32 soff) { return __builtin_amdgcn_raw_buffer_load_b128(r, voff, soff, 16); }
DI u32 ld32(rsrc_t r, u32 voff, u32 soff) { return __builtin_amdgcn_raw_buffer_load_b32(r, voff, soff, 16); }
DI void st128(rsrc_t r, u32 voff, u32 soff, u4v v) { __builtin_amdgcn_raw_buffer_store_b128(v, r, voff, soff, 16); }
DI void st64(rsrc_t r, u32 voff, u32 soff, u2v v) { __builtin_amdgcn_raw_buffer_store_b64(v, r, voff, soff, 16); }
DI void st32(rsrc_t r, u32 voff, u32 soff, u32 v) { __builtin_amdgcn_raw_buffer_store_b32(v, r, voff, soff, 16); }
DI float fmx(float a, float b) { return __builtin_elementwise_maximum(a, b); }
DI float fmn(float a, float b) { return __builtin_elementwise_minimum(a, b); }
DI void touch(u32 voff, const void* sbase, u32 lb) {
  asm volatile("s_mov_b32 m0, %2\n\ts_nop 0\n\tglobal_load_lds_dword %0, %1" ::"v"(voff), "s"(sbase), "s"(lb + L_DUMMY) : "memory");
}

DI void cnt_arrive(u32 addr, int lane) {
  asm volatile("s_waitcnt lgkmcnt(0)" ::: "memory");
  if (lane == 0) __hip_atomic_fetch_add((LAS u32*)(size_t)addr, 1u, __ATOMIC_RELAXED, __HIP_MEMORY_SCOPE_WORKGROUP);
}
DI void cnt_wait(u32 addr) {
#pragma unroll 1
  for (u32 spin = 0; spin < SPIN_CAP; ++spin)
  {
    if (__builtin_amdgcn_readfirstlane(__hip_atomic_load((LAS u32*)(size_t)addr, __ATOMIC_RELAXED, __HIP_MEMORY_SCOPE_WORKGROUP)) >= 4u) break;
    __builtin_amdgcn_s_sleep(1);
  }
  asm volatile("" ::: "memory");
}
DI u64 tag64(u32 t) { return (u64)t | ((u64)(t * 0x9E3779B1u + 0x7F4A7C15u) << 32); }

DI void flag_set(char* ws, u32 off, u64 tag, int lane) {
  asm volatile("s_waitcnt vmcnt(0)" ::: "memory");
  if (lane == 0) __hip_atomic_store((GAS u64*)(ws + off), tag, __ATOMIC_RELAXED, __HIP_MEMORY_SCOPE_AGENT);
}
DI void flag_wait(char* ws, u32 off, int n, u64 tag, int lane, u32 code) {
  GAS u64* ptr = (GAS u64*)(ws + off);
#pragma unroll 1
  for (u32 spin = 0;; ++spin) {
    u64 v = tag;
    if (lane < n) v = __hip_atomic_load(ptr, __ATOMIC_RELAXED, __HIP_MEMORY_SCOPE_AGENT);
    if (__all(v == tag)) break;
    if (spin > SPIN_CAP) {
      if (lane == 0) __hip_atomic_store((GAS u32*)(ws + WS_ERR), code, __ATOMIC_RELAXED, __HIP_MEMORY_SCOPE_AGENT);
      break;
    }
    __builtin_amdgcn_s_sleep(1);
  }
  asm volatile("" ::: "memory");
}
DI void stat_wait(char* ws, rsrc_t rs, int token, u64 tag, int lane, float& mean, float& sigma) {
#pragma unroll 1
  for (u32 spin = 0;; ++spin) {
    u4v g = ld128(rs, token * 16, OFF_ST);
    mean = __uint_as_float(g[0]);
    sigma = __uint_as_float(g[1]);
    bool ok = g[2] == (u32)tag && g[3] == (u32)(tag >> 32);
    if (__all(ok)) break;
    if (spin > SPIN_CAP) {
      if (lane == 0) __hip_atomic_store((GAS u32*)(ws + WS_ERR), 99u, __ATOMIC_RELAXED, __HIP_MEMORY_SCOPE_AGENT);
      break;
    }
    __builtin_amdgcn_s_sleep(1);
  }
}
DI void split2(float v, u32& hi, u32& lo) {
  hi = bf_rn(v);
  lo = bf_rn(v - __uint_as_float(hi << 16));
}
DI bf8 aug_w(float cs, u32 beta, int hh) {
  u32 hi, lo;
  split2(cs, hi, lo);
  u4v r = {hi | (hi << 16), lo | (beta << 16), beta, 0u};
  if (hh) r = u4v{0u, 0u, 0u, 0u};
  return as_bf8(r);
}

struct Role {
  int i, j;
  int jw;
  int m64, cc;
  int tq;
  bool bwork;
  int rho;
  int sidx;
};

DI u32 wslot(int g) { return (u32)((0x1076u >> (4 * (g & 3))) & 15) * SLOT; }
DI u32 hslot(int g) { return (u32)((0x3254u >> (4 * (g & 3))) & 15) * SLOT; }
DI constexpr u32 fslot(int c) { return (u32)(2 * (c % 4)) * SLOT; }
constexpr u32 L_KVA = 6 * SLOT;
constexpr u32 L_KVB = 0;
constexpr u32 L_Q = 2 * SLOT;
constexpr u32 L_HS = 4 * SLOT;

DI void loader_main(const Params& p, u32 lb, const Role& R, int lw, int lane, int wave) {
  const int i = R.i, j = R.j, m64 = R.m64, cc = R.cc;
  const u32 vx0 = (lane >> 3) * XROW + (((lane & 7) ^ (lane >> 4)) << 4);
  const u32 vx1 = (vx0 ^ 64) + (8 * XROW - 1024), vx2 = vx0 + 2 * (8 * XROW - 1024), vx3 = vx1 + 2 * (8 * XROW - 1024);
  const u32 vw0 = (lane >> 4) * W1ROW + (((lane & 15) ^ ((lane >> 4) << 2)) << 4);
  const u32 vw1 = vw0 + (4 * W1ROW - 1024), vw2 = vw0 + 2 * (4 * W1ROW - 1024), vw3 = vw0 + 3 * (4 * W1ROW - 1024);
  const char* xb = (const char*)p.x + (size_t)(128 * i + 32 * lw) * XROW;
  const char* w1b = (const char*)p.w1 + (size_t)(16 * lw) * W1ROW + R.jw * 256;
  const u32 lq = lb + lw * 4096;
  int gc = 0;
  auto stage = [&](u32 slot) {
    dma4(vx0, vx1, vx2, vx3, xb + gc * 128, lq + slot);
    dma4(vw0, vw1, vw2, vw3, w1b + (size_t)gc * (64 * W1ROW), lq + slot + SLOT);
    ++gc;
  };
  if (lw == 3) {
    const int t = R.sidx * 6 + min(lane, 5);
    const char* q;
    if (t < 28) q = (const char*)p.x + t * 65536;
    else if (t < 47) q = (const char*)p.wqkv + (t - 28) * 63000;
    else if (t < 48) q = (const char*)p.wphi;
    else if (t < 54) q = (const char*)p.wo + (t - 48) * 65536;
    else if (t < 79) q = (const char*)p.w1 + (t - 54) * 64000;
    else if (t < 80) q = (const char*)p.b1;
    else if (t < 105) q = (const char*)p.w2 + (t - 80) * 64000;
    else if (t < 106) q = (const char*)p.b2;
    else if (t < 134) q = (const char*)p.out + (t - 106) * 65536;
    else if (t < 166) q = p.ws + OFF_H + (size_t)(2 * (t - 134) + 1) * H_TILE - 128;
    else if (t < 174) q = p.ws + OFF_KVP + (size_t)(4 * (t - 166) + 1) * KVP_HS - 128;
    else if (t < 182) q = p.ws + OFF_O + (size_t)(8 * (t - 174) + 1) * O_TILE - 128;
    else q = p.ws + 512;
    asm volatile("s_mov_b32 m0, %1\n\ts_nop 0\n\tglobal_load_lds_dword %0, off" ::"v"(q), "s"(lb + L_DUMMY) : "memory");
  }
  auto warm = [&](int c0) {
    const int lx = min(lane, 8 * (7 - c0) - 1), lz = min(lane, 4 * (7 - c0) - 1);
    touch((32 * (j & 3) + 8 * lw + (lx & 7)) * XROW + (c0 + (lx >> 3)) * 128, (const char*)p.x + (size_t)(128 * i) * XROW, lb);
    touch((64 * (c0 + (lz >> 2)) + 8 * (i & 7) + 2 * lw + ((lz & 3) >> 1)) * W1ROW + (lz & 1) * 128, (const char*)p.w1 + R.jw * 256, lb);
  };
  bar_only();
  STAMP(12);
  stage(fslot(0));
  STAMP(13);
  warm(1);
  stage(fslot(1));
  const u32 E = *(CAS const u32*)(p.ws + WS_EPOCH);
  const u64 tag = tag64(E + 1);
  const rsrc_t rs = __builtin_amdgcn_make_buffer_rsrc(p.ws, 0, WS_USED, 0x00020000);
  const u32 vl = lane * 16;
  const u32 bchunk = ((lane & 7) ^ (((lane >> 4) & 1) << 2)) << 4;
  const u32 vb0 = (lane >> 3) * XROW + bchunk;
  const u32 vb1 = vb0 + (8 * XROW - 1024), vb2 = vb0 + 2 * (8 * XROW - 1024), vb3 = vb0 + 3 * (8 * XROW - 1024);
  const u32 vb3c = min(lane >> 3, 3) * XROW + bchunk + 3 * (8 * XROW - 1024);
  STAMP(1);
  vmwait<10>(); bar_only();
  STAMP(2);
  stage(fslot(2)); vmwait<8>(); bar_only();
  stage(fslot(3)); vmwait<8>(); bar_only();
  stage(fslot(4)); vmwait<8>(); bar_only();
  stage(fslot(5));
  const char* wp = p.ws + OFF_WP + (size_t)j * WP_HEAD;
  const u32 pflag = lane < 42 ? FL_P0 + (j * 48 + (lane / 14) * 16 + lane % 14) * FLS : FL_C1 + (R.jw * 16 + min(lane - 42, 15)) * FLS;
  dma_sc1(pflag, p.ws, lb + L_V + 4096 + lw * 1024);
  vmwait<9>(); bar_only();
  stage(fslot(6));
  vmwait<8>(); bar_only();
  STAMP(3);
  {
    const u2v f = *(LAS const u2v*)(size_t)(lb + L_V + 4096 + lw * 1024 + lane * 16);
    if (!__all(f[0] == (u32)tag && f[1] == (u32)(tag >> 32))) flag_wait(p.ws, pflag, 58, tag, lane, 1);
  }
  STAMP(4);
  const u32 gs = lb + L_PHI + lw * 4096;
  {
    const char* c1 = p.ws + OFF_C1P + (size_t)R.jw * 16 * 512 + lw * 128;
    const u32 vg = (lane >> 3) * 512 + (lane & 7) * 16;
    dma_lin<1>(vg, c1, gs);
    dma_lin<1>(vg, c1 + 8 * 512, gs + 1024);
    dma_lin<2>(vl, p.ws + OFF_CSP + (size_t)(j * 3 + min(lw, 2)) * 2048, gs + 2048);
  }
  asm volatile("s_mov_b32 m0, %2\n\ts_nop 0\n\tglobal_load_lds_dword %0, %1" ::"v"(min(lane, 15) * 4), "s"((const char*)p.b1 + (R.jw * 128 + lw * 32) * 2),
               "s"(lb + L_V + lw * 256) : "memory");
#pragma unroll
  for (int c = 0; c < 2; ++c) dma_lin<2>(vl, wp + c * 8192 + lw * 2048, lb + L_KVA + c * 8192 + lw * 2048);
  vmwait<9>(); bar_only();
  vmwait<4>();
  {
    const int hh = lane >> 5, l31 = lane & 31;
    LAS const float* src = (LAS const float*)(size_t)(gs + hh * 1024 + l31 * 4);
    float a = 0.f, b = 0.f;
#pragma unroll
    for (int q = 0; q < 8; ++q) a += src[32 * q];
    LAS const float* src2 = (LAS const float*)(size_t)(gs + 2048 + hh * 7 * 128 + l31 * 4);
#pragma unroll
    for (int q = 0; q < 7; ++q) b += src2[32 * q];
    const u32 beta1 = *(LAS const u16*)(size_t)(lb + L_V + lw * 256 + l31 * 2);
    LAS char* aug = (LAS char*)(size_t)(lb + L_AUG) + vl;
    *(LAS bf8*)(aug + lw * 1024) = aug_w(xhalf_sum(a), beta1, hh);
    if (lw < 3) *(LAS bf8*)(aug + (4 + lw) * 1024) = aug_w(xhalf_sum(b), (lw == 1 && l31 == 28) ? 0x3f80u : 0u, hh);
    asm volatile("s_waitcnt lgkmcnt(0)" ::: "memory");
  }
#pragma unroll
  for (int c = 2; c < 4; ++c) dma_lin<2>(vl, wp + c * 8192 + lw * 2048, lb + L_KVA + c * 8192 + lw * 2048);
#pragma unroll
  for (int c = 4; c < 7; ++c) dma_lin<2>(vl, wp + c * 8192 + lw * 2048, lb + L_KVB + (c - 4) * 8192 + lw * 2048);
  bar_only();
  STAMP(5);
#pragma unroll
  for (int c = 0; c < 7; ++c) dma_lin<1>(vl, wp + WP_Q + c * 4096 + lw * 1024, lb + L_Q + c * 4096 + lw * 1024);
  if (R.bwork) {
#pragma unroll 1
    for (int q = lw; q < 9; q += 4) {
      if (q < 7) touch(lane * XROW, (const char*)p.w2 + (size_t)(448 * R.tq + 64 * q) * XROW + cc * 128, lb);
      else touch(min(lane, q == 7 ? 63 : 47) * XROW, (const char*)p.wo + (size_t)(112 * R.tq + 64 * (q - 7)) * XROW + cc * 128, lb);
    }
    if (lw == 0) vmwait<10>(); else vmwait<9>();
  } else {
    vmwait<7>();
  }
  bar_only();
  if (j < 14) {
    LAS const char* src = (LAS const char*)(size_t)(lb + L_HS + lw * 8192) + vl;
    const u32 hb = OFF_H + (4 * i + lw) * H_TILE + 8 * j * 1024;
    u4v hv[8];
#pragma unroll
    for (int q = 0; q < 8; ++q) hv[q] = *(LAS const u4v*)(src + q * 1024);
#pragma unroll
    for (int q = 0; q < 8; ++q) st128(rs, vl, hb + q * 1024, hv[q]);
  }
  STAMP(14);
  vmwait<0>();
  if (j < 14 && lane == 0) __hip_atomic_store((GAS u64*)(p.ws + FL_H + ((i * 16 + j) * 4 + lw) * FLS), tag, __ATOMIC_RELAXED, __HIP_MEMORY_SCOPE_AGENT);
  cnt_arrive(lb + L_CNT + 8, lane);
  cnt_wait(lb + L_CNT + 4);
  STAMP(6);
  const char* w2p = (const char*)p.w2 + (size_t)(32 * lw) * XROW + cc * 128;
  auto issue_w = [&](int g) {
    if (g < 14) {
      dma4(vb0, vb1, vb2, vb3, w2p, lq + wslot(g));
      w2p += 128 * XROW;
    } else {
      dma4(vb0, vb1, vb2, vb3c, (const char*)p.wo + (size_t)(28 * (4 * (g - 14) + lw)) * XROW + cc * 128, lq + wslot(g));
    }
  };
  const char* hp = p.ws + OFF_H + (size_t)(2 * m64 + (lw >> 1)) * H_TILE + (lw & 1) * 4096;
  auto issue_h = [&](int g) {
    if (g == 14) hp = p.ws + OFF_O + (size_t)(2 * m64 + (lw >> 1)) * O_TILE + (lw & 1) * 4096;
    dma_lin<4>(vl, hp, lq + hslot(g));
    hp += 8192;
  };
  if (!R.bwork) return;
  issue_w(0);
  issue_w(1);
  issue_w(2);
  issue_w(3);
  flag_wait(p.ws, FL_H + (((m64 >> 1) * 16 + (lane >> 1)) * 4 + 2 * (m64 & 1) + (lane & 1)) * FLS, 28, tag, lane, 2);
  STAMP(7);
  cnt_wait(lb + L_CNT + 8);
  issue_h(0);
  touch(lane * 128, p.ws + OFF_H + (size_t)(2 * m64 + (lw >> 1)) * H_TILE + (16 * cc + 8 * (lw & 1)) * 1024, lb);
  issue_h(1);
  vmwait<5>(); bar_only();
  issue_h(2);
  vmwait<4>(); bar_only();
  issue_h(3);
  {
    const char* b = (const char*)p.x + (size_t)(64 * m64 + 16 * lw) * XROW + cc * 128;
    dma(vx0, b, lb + L_V + lw * 2048);
    dma(vx0 ^ 64, b + 8 * XROW, lb + L_V + lw * 2048 + 1024);
  }
  STAMP(8);
  vmwait<6>(); bar_only();
#pragma unroll 1
  for (int g = 2; g < 12; ++g) {
    issue_h(g + 2);
    issue_w(g + 2);
    vmwait<8>();
    bar_only();
  }
  issue_w(14);
  vmwait<4>(); bar_only();
  issue_w(15);
  dma4(vb0, vb1, vb2, vb3c, (const char*)p.wo + (size_t)(28 * (8 + lw)) * XROW + cc * 128, lb + L_PHI + lw * 4096);
  flag_wait(p.ws, FL_O + (((m64 >> 1) * 16 + (lane >> 1)) * 4 + 2 * (m64 & 1) + (lane & 1)) * FLS, 32, tag, lane, 3);
  STAMP(9);
  issue_h(14);
  issue_h(15);
  {
    const int line = min((4 * cc + lw) * 19 + min(lane, 18), 511);
    touch(line * 128 + (line >> 8) * 128, p.ws + OFF_O + (size_t)(2 * m64) * O_TILE, lb);
  }
  vmwait<5>(); bar_only();
  issue_h(16);
  issue_w(17);
  issue_h(17);
  vmwait<12>(); bar_only();
  vmwait<8>(); bar_only();
  vmwait<0>(); bar_only();
  STAMP(10);
  if (blockIdx.x == 0 && lw == 0 && lane == 0)
    __hip_atomic_store((GAS u32*)(p.ws + WS_EPOCH), E + 1, __ATOMIC_RELAXED, __HIP_MEMORY_SCOPE_AGENT);
  STAMP(11);
}

DI void exp_pair(const f16v& a, float c, f16v& ep, f16v& en) {
  float mx = fmx(a[0], a[1]), mn = fmn(a[0], a[1]);
#pragma unroll
  for (int r = 2; r < 16; r += 2) {
    mx = fmx(fmx(mx, a[r]), a[r + 1]);
    mn = fmn(fmn(mn, a[r]), a[r + 1]);
  }
  mx = xhalf_max(mx);
  mn = xhalf_min(mn);
  const f2v c2 = {c, c}, bp = {-mx * c, -mx * c}, bn = {mn * c, mn * c};
#pragma unroll
  for (int r = 0; r < 16; r += 2) {
    const f2v tp = __builtin_elementwise_fma(pair(a, r), c2, bp), tn = __builtin_elementwise_fma(pair(a, r), -c2, bn);
    ep[r] = __builtin_amdgcn_exp2f(tp[0]); ep[r + 1] = __builtin_amdgcn_exp2f(tp[1]);
    en[r] = __builtin_amdgcn_exp2f(tn[0]); en[r + 1] = __builtin_amdgcn_exp2f(tn[1]);
  }
}
DI float sum28(const f16v& e, int hh) {
  const f2v s0 = (pair(e, 0) + pair(e, 2)) + (pair(e, 4) + pair(e, 6)), s1 = pair(e, 8) + pair(e, 10), s2 = pair(e, 12) + pair(e, 14);
  const f2v t = s0 + s1;
  return xhalf_sum((t[0] + t[1]) + (hh ? 0.f : s2[0] + s2[1]));
}

DI void fold_load(const Params& p, u32 lb, int i, int head, int part, int lane, u4v (&a)[2]) {
  const int r16 = lane & 15, dg = lane >> 4;
  const int col = part == 0 ? XROW : (part == 1 ? 2 * XROW : 0);
  const char* src = (const char*)p.wqkv + (size_t)(32 * i + r16) * QROW + col + head * 56 + (dg == 3 ? 40 : dg * 16);
  a[0] = *(GAS const u4v*)src;
  a[1] = *(GAS const u4v*)(src + 16 * QROW);
  const char* wp = (const char*)p.wphi + head * 1568;
  const u32 dst = lb + L_PHI + part * 2048;
  dma(lane * 16, wp, dst);
  dma(min(lane, 33) * 16, wp + 1024, dst + 1024);
}
DI void fold_run(rsrc_t rs, LAS const char* lds, int i, int head, int part, int lane, const u4v (&a)[2]) {
  const int dg = lane >> 4, r16 = lane & 15;
  asm volatile("s_waitcnt vmcnt(0)" ::: "memory");
  u4v bw[2];
  {
    const int q4 = r16 >> 2, pp = lane & 3;
    LAS const char* b = lds + L_PHI + part * 2048 + (8 * dg + q4) * 56 + pp * 8;
#pragma unroll
    for (int ft = 0; ft < 2; ++ft) {
      u4v v = __builtin_bit_cast(u4v, lds_tr2(b + ft * 32, b + ft * 32 + 4 * 56));
      if (dg == 3) { v[2] = 0u; v[3] = 0u; }
      if (part == 1) {
        const int t = 16 * ft + r16 - 8 * dg;
        const u32 one = (16 * ft + r16 < 28) ? ((t & 1) ? 0x3f800000u : 0x3f80u) : 0u;
#pragma unroll
        for (int q = 0; q < 4; ++q) v[q] = (t >> 1) == q ? one : 0u;
      }
      bw[ft] = v;
    }
  }
  const u32 base = OFF_WP + head * WP_HEAD + (part == 2 ? WP_Q + 2 * i * 1024 : 2 * i * 2048 + part * 1024);
  const u32 step = part == 2 ? 1024 : 2048;
  float cs = 0.f;
#pragma unroll
  for (int t = 0; t < 2; ++t) {
    u4v av = a[t];
    if (dg == 3) av = u4v{av[2], av[3], 0u, 0u};
    const f4v z = {0.f, 0.f, 0.f, 0.f};
    f4v d0 = mfma16(as_bf8(av), as_bf8(bw[0]), z), d1 = mfma16(as_bf8(av), as_bf8(bw[1]), z);
    if (part != 1) {
#pragma unroll
      for (int r = 0; r < 4; ++r) {
        const int v = __float_as_int(d1[r]);
        d1[r] = __int_as_float(__builtin_amdgcn_update_dpp(v, v, 0x15B, 0xf, 0x8, false));
      }
    }
    auto s0 = __builtin_amdgcn_permlane16_swap(pk2(d0[0], d0[1]), pk2(d1[0], d1[1]), false, false);
    auto s1 = __builtin_amdgcn_permlane16_swap(pk2(d0[2], d0[3]), pk2(d1[2], d1[3]), false, false);
    const u4v o = {s0[0], s1[0], s0[1], s1[1]};
    st128(rs, lane * 16, base + t * step, o);
    cs += ((bf_lo(o[0]) + bf_hi(o[0])) + (bf_lo(o[1]) + bf_hi(o[1]))) + ((bf_lo(o[2]) + bf_hi(o[2])) + (bf_lo(o[3]) + bf_hi(o[3])));
  }
  cs = xhalf_sum(cs);
  if (lane < 32) st32(rs, lane * 4, OFF_CSP + ((head * 3 + part) * 16 + i) * 128, __float_as_uint(cs));
}

constexpr int FD = 6, FNB = FD + 1;
template <int U>
DI void f_request(LAS const char* lds, u32 (&xa)[4], u32 (&wa)[4], bf8 (&x)[7][4], bf8 (&wf)[FNB]) {
  if constexpr (U < 112) {
    constexpr int c = U >> 4, s = (U >> 2) & 3, n = U & 3;
    if constexpr ((U & 31) == 0 && U > 0) {
#pragma unroll
      for (int q = 0; q < 4; ++q) {
        xa[q] ^= 65536u;
        wa[q] ^= 65536u;
        asm volatile("" : "+v"(xa[q]), "+v"(wa[q]));
      }
    }
    constexpr u32 xs = fslot(c) & 65535u, ws = xs + SLOT;
    if constexpr (n == 0) x[c][s] = lds_frag(lds + xs + xa[s]);
    wf[U % FNB] = lds_tr2(lds + ws + wa[n] + 4096 * s, lds + ws + wa[n] + 4096 * s + 1024);
  }
}
template <int C, int V>
DI void f_unit(LAS const char* lds, u32 (&xa)[4], u32 (&wa)[4], bf8 (&x)[7][4], bf8 (&wf)[FNB], f16v (&acc)[4]) {
  constexpr int U = 16 * C + V;
  if constexpr (V == 16 - FD && C < 6) bar_only();
  f_request<U + FD>(lds, xa, wa, x, wf);
  acc[V & 3] = mfma32(wf[U % FNB], x[C][V >> 2], acc[V & 3]);
  SCHED();
  if constexpr (V < 15) f_unit<C, V + 1>(lds, xa, wa, x, wf, acc);
}
template <int C>
DI void tick_f(LAS const char* lds, u32 (&xa)[4], u32 (&wa)[4], bf8 (&x)[7][4], bf8 (&wf)[FNB], f16v (&acc)[4]) {
  f_unit<C, 0>(lds, xa, wa, x, wf, acc);
}
template <int U>
DI void f_prime(LAS const char* lds, u32 (&xa)[4], u32 (&wa)[4], bf8 (&x)[7][4], bf8 (&wf)[FNB]) {
  f_request<U>(lds, xa, wa, x, wf);
  if constexpr (U + 1 < FD) f_prime<U + 1>(lds, xa, wa, x, wf);
}

DI void compute_main(const Params& p, LAS char* lds, const Role& R, int w, int lane, int wave) {
  const rsrc_t rs = __builtin_amdgcn_make_buffer_rsrc(p.ws, 0, WS_USED, 0x00020000);
  const int i = R.i, j = R.j;
  const int hh = lane >> 5, l31 = lane & 31;
  const bool ffn = j < 14;
  const bool fold = i < 14 && w < 3;
  const u32 E = *(CAS const u32*)(p.ws + WS_EPOCH);
  const u64 tag = tag64(E + 1);
  u32 xs7[7];
  const int stok = 128 * i + 8 * j + 2 * w + hh;
#pragma unroll
  for (int m = 0; m < 7; ++m) xs7[m] = *(GAS const u32*)((const char*)p.x + (size_t)stok * XROW + m * 128 + l31 * 4);
  auto stats = [&]() {
    f2v s2 = {0.f, 0.f};
#pragma unroll
    for (int m = 0; m < 7; ++m) s2 += f2v{bf_lo(xs7[m]), bf_hi(xs7[m])};
    const float mean = half_sum(s2[0] + s2[1]) * (1.f / D_);
    f2v q2 = {0.f, 0.f};
#pragma unroll
    for (int m = 0; m < 7; ++m) {
      const f2v a = f2v{bf_lo(xs7[m]), bf_hi(xs7[m])} - mean;
      q2 = __builtin_elementwise_fma(a, a, q2);
    }
    const float sigma = __builtin_amdgcn_sqrtf(half_sum(q2[0] + q2[1]) * (1.f / D_) + 1e-5f);
    if (l31 == 0) st128(rs, stok * 16, OFF_ST, u4v{__float_as_uint(mean), __float_as_uint(sigma), (u32)tag, (u32)(tag >> 32)});
  };
  if (fold) {
    u4v fa[2];
    fold_load(p, (u32)(size_t)lds, i, j, w, lane, fa);
    SCHED();
    bar_only();
    SCHED();
    STAMP(22);
    fold_run(rs, lds, i, j, w, lane, fa);
    stats();
  } else if (w < 3) {
    bar_only();
    stats();
  } else {
    u4v fc[7];
    const char* src = (const char*)p.w1 + (size_t)(28 * i + (lane >> 4)) * W1ROW + R.jw * 256 + (lane & 15) * 16;
#pragma unroll
    for (int q = 0; q < 7; ++q) fc[q] = *(GAS const u4v*)(src + (size_t)q * 4 * W1ROW);
    SCHED();
    bar_only();
    SCHED();
    float cs[8];
#pragma unroll
    for (int e = 0; e < 8; ++e) cs[e] = 0.f;
#pragma unroll
    for (int q = 0; q < 7; ++q)
#pragma unroll
      for (int e = 0; e < 4; ++e) { cs[2 * e] += bf_lo(fc[q][e]); cs[2 * e + 1] += bf_hi(fc[q][e]); }
#pragma unroll
    for (int e = 0; e < 8; ++e) {
      auto r = __builtin_amdgcn_permlane16_swap(__float_as_uint(cs[e]), __float_as_uint(cs[e]), false, false);
      cs[e] = xhalf_sum(__uint_as_float(r[0]) + __uint_as_float(r[1]));
    }
    if (ffn && lane < 32) {
      const int o = (lane >> 4) * 4;
      st128(rs, (lane & 15) * 32 + (lane >> 4) * 16, OFF_C1P + (j * 16 + i) * 512,
            u4v{__float_as_uint(o ? cs[4] : cs[0]), __float_as_uint(o ? cs[5] : cs[1]), __float_as_uint(o ? cs[6] : cs[2]), __float_as_uint(o ? cs[7] : cs[3])});
    }
    stats();
  }
  STAMP(23);
  u32 xa[4], wa[4];
  {
    const int row = 32 * w + l31;
    const u32 b = row * 128 + ((hh ^ ((row >> 1) & 7)) << 4);
#pragma unroll
    for (int s = 0; s < 4; ++s) xa[s] = b ^ (32 * s);
    const int q4 = (lane & 15) >> 2, pp = lane & 3, blk = (lane >> 4) & 1;
#pragma unroll
    for (int n = 0; n < 4; ++n) {
      const int nt = (n + w) & 3;
      wa[n] = (8 * hh + q4) * 256 + (4 * (nt ^ q4) + 2 * blk + (pp >> 1)) * 16 + 8 * (pp & 1);
    }
  }
  bf8 x[7][4], wf[FNB];
  f16v acc[4];
#pragma unroll
  for (int n = 0; n < 4; ++n)
#pragma unroll
    for (int r = 0; r < 16; ++r) acc[n][r] = 0.f;

  if (w == 0 && lane < 8) *(LAS volatile u32*)(lds + L_CNT + lane * 4) = 0u;
  bar();
  STAMP(2);
  f_prime<0>(lds, xa, wa, x, wf);
  tick_f<0>(lds, xa, wa, x, wf, acc);
  STAMP(30);
  vmwait<1>();
  if (lane == 0) {
    if (fold) __hip_atomic_store((GAS u64*)(p.ws + FL_P0 + ((j * 3 + w) * 16 + i) * FLS), tag, __ATOMIC_RELAXED, __HIP_MEMORY_SCOPE_AGENT);
    if (ffn && w == 3) __hip_atomic_store((GAS u64*)(p.ws + FL_C1 + (j * 16 + i) * FLS), tag, __ATOMIC_RELAXED, __HIP_MEMORY_SCOPE_AGENT);
  }
  tick_f<1>(lds, xa, wa, x, wf, acc);
  STAMP(31);
  const int token = 128 * i + 32 * w + l31;
  u4v sg = ld128(rs, token * 16, OFF_ST);
  tick_f<2>(lds, xa, wa, x, wf, acc);
  STAMP(32);
  tick_f<3>(lds, xa, wa, x, wf, acc);
  STAMP(33);
  float mean = __uint_as_float(sg[0]), sigma = __uint_as_float(sg[1]);
  if (!__all(sg[2] == (u32)tag && sg[3] == (u32)(tag >> 32))) stat_wait(p.ws, rs, token, tag, lane, mean, sigma);
  tick_f<4>(lds, xa, wa, x, wf, acc);
  STAMP(34);
  bf8 xaug;
  {
    u32 mh, ml, sh, sl;
    split2(-mean, mh, ml);
    split2(sigma, sh, sl);
    u4v r = {mh | (ml << 16), mh | (sh << 16), sl, 0u};
    if (hh) r = u4v{0u, 0u, 0u, 0u};
    xaug = as_bf8(r);
  }
  const float rstd = __builtin_amdgcn_rcpf(sigma);
  tick_f<5>(lds, xa, wa, x, wf, acc);
  STAMP(35);
  tick_f<6>(lds, xa, wa, x, wf, acc);
  STAMP(4);
  bar();
  STAMP(6);
  const u32 vl = lane * 16;
  if (ffn) {
#pragma unroll
    for (int n = 0; n < 4; ++n) acc[n] = mfma32(lds_frag(lds + L_AUG + ((n + w) & 3) * 1024 + vl), xaug, acc[n]);
    LAS char* hb = lds + L_HS + w * 8192 + vl;
#pragma unroll
    for (int n = 0; n < 4; ++n) {
#pragma unroll
      for (int r = 0; r < 16; r += 2) {
        const f2v t = {fmx(acc[n][r], 0.f), fmx(acc[n][r + 1], 0.f)};
        const f2v u = t * t;
        acc[n][r] = u[0];
        acc[n][r + 1] = u[1];
      }
#pragma unroll
      for (int s = 0; s < 2; ++s) *(LAS bf8*)(hb + (2 * ((n + w) & 3) + s) * 1024) = frag8(acc[n], s);
    }
  }
  bar();
  STAMP(5);
  f16v ak, av;
#pragma unroll
  for (int r = 0; r < 16; ++r) { ak[r] = 0.f; av[r] = 0.f; }
  {
    u32 kva = vl + L_KVA;
    asm volatile("" : "+v"(kva));
    LAS const char* wa_ = lds + kva;
    LAS const char* wb_ = lds + vl + L_KVB;
    auto kvp = [&](int ks) -> LAS const char* { return ks < 16 ? wa_ + ks * 2048 : (ks < 28 ? wb_ + (ks - 16) * 2048 : wa_ + (L_AUG + 4 * 1024 - L_KVA)); };
    bf8 fk[4], fv[4];
#pragma unroll
    for (int ks = 0; ks < 3; ++ks) {
      fk[ks] = lds_frag(kvp(ks));
      fv[ks] = lds_frag(kvp(ks) + 1024);
    }
    SCHED();
#pragma unroll
    for (int ks = 0; ks < 29; ++ks) {
      if (ks + 3 < 29) {
        fk[(ks + 3) & 3] = lds_frag(kvp(ks + 3));
        fv[(ks + 3) & 3] = lds_frag(kvp(ks + 3) + 1024);
      }
      const bf8 xb = ks < 28 ? x[(ks < 28 ? ks : 0) >> 2][ks & 3] : xaug;
      ak = mfma32(fk[ks & 3], xb, ak);
      av = mfma32(fv[ks & 3], xb, av);
      __builtin_amdgcn_sched_group_barrier(0x8, 1, 0);
      __builtin_amdgcn_sched_group_barrier(0x100, 1, 0);
      __builtin_amdgcn_sched_group_barrier(0x8, 1, 0);
      __builtin_amdgcn_sched_group_barrier(0x100, 1, 0);
      SCHED();
    }
  }
  cnt_arrive((u32)(size_t)lds + L_CNT + 4, lane);
  STAMP(7);
  const float cexp = rstd * LOG2E;
  {
    f16v ep, en;
    exp_pair(ak, cexp, ep, en);
    const float ap = rstd * __builtin_amdgcn_rcpf(sum28(ep, hh)), an = rstd * __builtin_amdgcn_rcpf(sum28(en, hh));
    const int row = 32 * w + l31;
    LAS char* base = lds + row * 64 + 8 * hh;
    const int key = (l31 >> 1) & 3;
#pragma unroll
    for (int q = 0; q < 4; ++q) {
      const u32 o = (q ^ key) << 4;
      *(LAS u2v*)(base + L_PHI + o) = u2v{pk2v(pair(ep, 4 * q) * ap), pk2v(pair(ep, 4 * q + 2) * ap)};
      *(LAS u2v*)(base + L_PHI + 8192 + o) = u2v{pk2v(pair(en, 4 * q) * an), pk2v(pair(en, 4 * q + 2) * an)};
      *(LAS u2v*)(base + L_V + o) = u2v{pk2(av[4 * q], av[4 * q + 1]), pk2(av[4 * q + 2], av[4 * q + 3])};
    }
  }
  STAMP(8);
  cnt_arrive((u32)(size_t)lds + L_CNT, lane);
  if (w < 2) {
    cnt_wait((u32)(size_t)lds + L_CNT);
    STAMP(9);
    const int q4 = (lane & 15) >> 2, pp = lane & 3, blk = (lane >> 4) & 1;
    u32 ta[2];
#pragma unroll
    for (int t = 0; t < 2; ++t) {
      const int row = 8 * hh + 4 * t + q4;
      ta[t] = row * 64 + (((2 * blk + (pp >> 1)) ^ ((row >> 1) & 3)) << 4) + 8 * (pp & 1);
    }
    f16v kv;
#pragma unroll
    for (int r = 0; r < 16; ++r) kv[r] = 0.f;
    LAS const char* pa = lds + L_PHI + w * 8192;
    LAS const char* pb = lds + L_V;
#pragma unroll
    for (int ks = 0; ks < 8; ++ks)
      kv = mfma32(lds_tr2(pa + ta[0] + ks * 1024, pa + ta[1] + ks * 1024), lds_tr2(pb + ta[0] + ks * 1024, pb + ta[1] + ks * 1024), kv);
    const u32 o = OFF_KVP + (j * 2 + w) * KVP_HS + i * 2048;
    st128(rs, vl, o, __builtin_bit_cast(u4v, frag8(kv, 0)));
    st128(rs, vl, o + 1024, __builtin_bit_cast(u4v, frag8(kv, 1)));
  }
  STAMP(18);
  cnt_wait((u32)(size_t)lds + L_CNT + 8);
  f16v aq;
#pragma unroll
  for (int r = 0; r < 16; ++r) aq[r] = 0.f;
  {
    LAS const char* wb = lds + L_Q + vl;
    bf8 fq[6];
#pragma unroll
    for (int ks = 0; ks < 5; ++ks) fq[ks] = lds_frag(wb + ks * 1024);
    SCHED();
#pragma unroll
    for (int ks = 0; ks < 29; ++ks) {
      if (ks + 5 < 28) fq[(ks + 5) % 6] = lds_frag(wb + (ks + 5) * 1024);
      else if (ks + 5 == 28) fq[(ks + 5) % 6] = lds_frag(lds + L_AUG + 6 * 1024 + vl);
      aq = mfma32(fq[ks % 6], ks < 28 ? x[(ks < 28 ? ks : 0) >> 2][ks & 3] : xaug, aq);
      SCHED();
      if (ks == 15 && w < 2) flag_set(p.ws, FL_KV + ((j * 2 + w) * 16 + i) * FLS, tag, lane);
    }
  }
  STAMP(10);
  bf8 eq[2][2];
  auto qepi = [&]() {
    f16v ep, en;
    exp_pair(aq, cexp, ep, en);
    eq[0][0] = frag8(ep, 0); eq[0][1] = frag8(ep, 1);
    eq[1][0] = frag8(en, 0); eq[1][1] = frag8(en, 1);
  };
  const int m64 = R.m64, cc = R.cc;
  const int bm = w & 1, bn = w >> 1;
  u32 ha, wb;
  {
    const int q4 = (lane & 15) >> 2, pp = lane & 3, blk = (lane >> 4) & 1;
    ha = bm * 8192 + bn * 4096 + vl;
    wb = bn * 8192 + (4 * hh + q4) * 128 + (4 * (q4 >> 1) + 2 * blk + (pp >> 1)) * 16 + 8 * (pp & 1);
  }
  const u4v sg2 = ld128(rs, (64 * m64 + 32 * bm + l31) * 16, OFF_ST);
  const u32 b2 = *((const GAS u16*)p.b2 + cc * 64 + bn * 32 + l31);
  f16v a0, a1;
#pragma unroll
  for (int r = 0; r < 16; ++r) { a0[r] = 0.f; a1[r] = 0.f; }
  bf8 fh[4], f0[4], f1[4];
  u32 hp = 0, wp = 0;
  auto point = [&](int g) { hp = hslot(g) + ha; wp = (g == 16 ? L_PHI : wslot(g)) + wb; };
  auto req = [&](int ks) {
    fh[ks] = lds_frag(lds + hp + ks * 1024);
    f0[ks] = lds_tr2(lds + wp + ks * 2048, lds + wp + ks * 2048 + 1024);
    f1[ks] = lds_tr2(lds + (wp ^ 64) + ks * 2048, lds + (wp ^ 64) + ks * 2048 + 1024);
  };
  auto mm = [&](int ks) {
    a0 = mfma32(f0[ks], fh[ks], a0);
    a1 = mfma32(f1[ks], fh[ks], a1);
    __builtin_amdgcn_sched_group_barrier(0x8, 1, 0);
    __builtin_amdgcn_sched_group_barrier(0x100, 3, 0);
    __builtin_amdgcn_sched_group_barrier(0x8, 1, 0);
    __builtin_amdgcn_sched_group_barrier(0x100, 2, 0);
    SCHED();
  };
  auto tick = [&](int g) {
    req(3); mm(0);
    bar_only();
    point(g + 1);
    req(0); mm(1);
    req(1); mm(2);
    req(2); mm(3);
  };
  const u32 krflag = FL_KR + (j * 8 + min(lane, 7)) * FLS;
  int g = 0;
  u4v kt[4];
  auto oload = [&]() {
#pragma unroll
    for (int q = 0; q < 4; ++q) kt[q] = ld128(rs, vl, OFF_KVT + (j * 4 + q) * 1024);
  };
  auto ostore = [&]() {
    f16v np, nn;
#pragma unroll
    for (int r = 0; r < 16; ++r) { np[r] = 0.f; nn[r] = 0.f; }
    np = mfma32(as_bf8(kt[0]), eq[0][0], np);
    nn = mfma32(as_bf8(kt[2]), eq[1][0], nn);
    np = mfma32(as_bf8(kt[1]), eq[0][1], np);
    nn = mfma32(as_bf8(kt[3]), eq[1][1], nn);
    const float ia = __builtin_amdgcn_rcpf(xhalf_hi(np[13])), ib = __builtin_amdgcn_rcpf(xhalf_hi(nn[13]));
    const float den = xhalf_hi(np[12]) * ia + xhalf_hi(nn[12]) * ib + 1e-6f;
    const float id = __builtin_amdgcn_rcpf(den), fa = ia * id, fb = ib * id;
    u32 ov[8];
#pragma unroll
    for (int r = 0; r < 16; r += 2) ov[r >> 1] = pk2v(pair(np, r) * fa + pair(nn, r) * fb);
    if (hh) { ov[6] = 0u; ov[7] = 0u; }
    const u32 ob = OFF_O + (4 * i + w) * O_TILE + 2 * j * 1024;
    st128(rs, vl, ob, u4v{ov[0], ov[1], ov[2], ov[3]});
    st128(rs, vl, ob + 1024, u4v{ov[4], ov[5], ov[6], ov[7]});
  };
  const u32 oflag = FL_O + ((i * 16 + j) * 4 + w) * FLS;
  if (R.bwork) {
    bar();
    point(0);
    req(0); req(1); req(2);
    SCHED();
    tick(0);
    tick(1);
    g = 2;
    qepi();
    SCHED();
    u64 kr = 0;
    bool ready = false;
#pragma unroll 1
    while (g < 10) {
      tick(g);
      ++g;
      if (__all(kr == tag)) { ready = true; break; }
      if (g >= 4) kr = __hip_atomic_load((GAS u64*)(p.ws + krflag), __ATOMIC_RELAXED, __HIP_MEMORY_SCOPE_AGENT);
    }
    if (!ready) flag_wait(p.ws, krflag, 8, tag, lane, 6);
    STAMP(14);
    oload();
    tick(g); ++g;
    ostore();
    tick(g); ++g;
    flag_set(p.ws, oflag, tag, lane);
    STAMP(15);
  } else {
    const int hs = R.rho, sk = w & 1, hf = w >> 1;
    flag_wait(p.ws, FL_KV + (hs * 16 + lane) * FLS, 16, tag, lane, 5);
    STAMP(12);
    const u32 o = OFF_KVP + hs * KVP_HS + sk * 1024 + hf * 512;
    u4v t[8];
#pragma unroll
    for (int q = 0; q < 8; ++q) t[q] = ld128(rs, hh * 8 * 2048 + l31 * 16, o + q * 2048);
    f2v sm[4];
#pragma unroll
    for (int e = 0; e < 4; ++e) sm[e] = f2v{bf_lo(t[0][e]), bf_hi(t[0][e])};
#pragma unroll
    for (int q = 1; q < 8; ++q)
#pragma unroll
      for (int e = 0; e < 4; ++e) sm[e] += f2v{bf_lo(t[q][e]), bf_hi(t[q][e])};
    u4v r;
#pragma unroll
    for (int e = 0; e < 4; ++e) r[e] = pk2(xhalf_sum(sm[e][0]), xhalf_sum(sm[e][1]));
    if (l31 == 29) r = u4v{0x3f803f80u, 0x3f803f80u, 0x3f803f80u, 0x3f803f80u};
    if (l31 >= 30) r = u4v{0u, 0u, 0u, 0u};
    if (sk == 1 && hf == 1) { r[2] = 0u; r[3] = 0u; }
    if (lane < 32) st128(rs, l31 * 16, OFF_KVT + (hs * 2 + sk) * 1024 + hf * 512, r);
    flag_set(p.ws, FL_KR + ((hs * 2 + sk) * 2 + hf) * FLS, tag, lane);
    STAMP(13);
    qepi();
    flag_wait(p.ws, krflag, 8, tag, lane, 6);
    STAMP(14);
    oload();
    ostore();
    flag_set(p.ws, oflag, tag, lane);
    STAMP(15);
    return;
  }
#pragma unroll 1
  for (; g < 13; ++g) tick(g);
  req(3); mm(0); mm(1); mm(2); mm(3);
  STAMP(24);
  float sil[16];
  u2v xr[4];
  {
    const u32 cb = (u32)(size_t)lds + L_CNT;
    cnt_arrive(cb + 12, lane);
    cnt_wait(cb + 12);
    LAS char* dst = lds + 6 * SLOT + (bm * 2 + (1 - bn)) * 4096 + vl;
    LAS const char* src = lds + 6 * SLOT + (bm * 2 + bn) * 4096 + vl;
#pragma unroll
    for (int q = 0; q < 4; ++q)
      *(LAS f4v*)(dst + q * 1024) = bn ? f4v{a0[4 * q], a0[4 * q + 1], a0[4 * q + 2], a0[4 * q + 3]} : f4v{a1[4 * q], a1[4 * q + 1], a1[4 * q + 2], a1[4 * q + 3]};
    cnt_arrive(cb + 16, lane);
    float m2 = __uint_as_float(sg2[0]), s2 = __uint_as_float(sg2[1]);
    if (!__all(sg2[2] == (u32)tag && sg2[3] == (u32)(tag >> 32))) stat_wait(p.ws, rs, 64 * m64 + 32 * bm + l31, tag, lane, m2, s2);
    const float var = s2 * s2;
    u32 vh, vlo;
    split2(var, vh, vlo);
    u4v xa2 = {vh | (vlo << 16), 0u, 0u, 0u};
    u4v wa3 = {b2 | (b2 << 16), 0u, 0u, 0u};
    if (hh) { xa2[0] = 0u; wa3[0] = 0u; }
    f16v ag = bn ? a1 : a0;
    ag = mfma32(as_bf8(wa3), as_bf8(xa2), ag);
    const float r2 = __builtin_amdgcn_rcpf(var);
    {
      const int row = 32 * bm + l31, key = (row >> 1) & 7;
      LAS const char* base = lds + L_V + row * 128 + 8 * hh;
#pragma unroll
      for (int q = 0; q < 4; ++q) xr[q] = *(LAS const u2v*)(base + (((4 * bn + q) ^ key) << 4));
    }
    cnt_wait(cb + 16);
#pragma unroll
    for (int q = 0; q < 4; ++q) {
      const f4v gv = *(LAS const f4v*)(src + q * 1024);
#pragma unroll
      for (int e = 0; e < 4; ++e) {
        const float gp = (ag[4 * q + e] + gv[e]) * r2;
        sil[4 * q + e] = gp * __builtin_amdgcn_rcpf(1.f + __builtin_amdgcn_exp2f(-gp * LOG2E));
      }
    }
  }
  f16v as;
#pragma unroll
  for (int r = 0; r < 16; ++r) as[r] = 0.f;
  {
    const int q4 = (lane & 15) >> 2, pp = lane & 3, blk = (lane >> 4) & 1;
    const u32 hs = bm * 8192 + vl, ws = (4 * hh + q4) * 128 + (4 * (bn ^ (q4 >> 1)) + 2 * blk + (pp >> 1)) * 16 + 8 * (pp & 1);
    bf8 sh[2][8], sw[2][8];
    auto rd = [&](int set, u32 hslot_, u32 wslot_) {
      LAS const char* hp_ = lds + hslot_ + hs;
      LAS const char* wp_ = lds + wslot_ + ws;
#pragma unroll
      for (int ks = 0; ks < 8; ++ks) {
        sh[set][ks] = lds_frag(hp_ + ks * 1024);
        sw[set][ks] = lds_tr2(wp_ + ks * 2048, wp_ + ks * 2048 + 1024);
      }
    };
    auto ms = [&](int set) {
#pragma unroll
      for (int ks = 0; ks < 8; ++ks) as = mfma32(sw[set][ks], sh[set][ks], as);
    };
    auto mix = [&]() {
#pragma unroll
      for (int ks = 0; ks < 8; ++ks) {
        __builtin_amdgcn_sched_group_barrier(0x8, 1, 0);
        __builtin_amdgcn_sched_group_barrier(0x100, 3, 0);
      }
      SCHED();
    };
    bar_only();
    rd(0, 2 * SLOT, 0 * SLOT);
    SCHED();
    bar_only();
    rd(1, 3 * SLOT, 1 * SLOT); ms(0); mix();
    bar_only();
    rd(0, 4 * SLOT, L_PHI); ms(1); mix();
    bar_only();
    rd(1, 5 * SLOT, 7 * SLOT); ms(0); mix();
    ms(1);
  }
  STAMP(16);
  {
    u32 ov[4][2];
#pragma unroll
    for (int q = 0; q < 4; ++q) {
      const float o0 = __builtin_fmaf(as[4 * q], sil[4 * q], bf_lo(xr[q][0])), o1 = __builtin_fmaf(as[4 * q + 1], sil[4 * q + 1], bf_hi(xr[q][0]));
      const float o2 = __builtin_fmaf(as[4 * q + 2], sil[4 * q + 2], bf_lo(xr[q][1])), o3 = __builtin_fmaf(as[4 * q + 3], sil[4 * q + 3], bf_hi(xr[q][1]));
      ov[q][0] = pk2(o0, o1);
      ov[q][1] = pk2(o2, o3);
    }
    char* orow = (char*)p.out + (size_t)(64 * m64 + 32 * bm + l31) * XROW + cc * 128 + bn * 64 + hh * 16;
#pragma unroll
    for (int q = 0; q < 4; q += 2) {
      auto r0 = __builtin_amdgcn_permlane32_swap(ov[q][0], ov[q + 1][0], false, false);
      auto r1 = __builtin_amdgcn_permlane32_swap(ov[q][1], ov[q + 1][1], false, false);
      *(GAS u4v*)(orow + q * 16) = u4v{r0[0], r1[0], r0[1], r1[1]};
    }
  }
  STAMP(17);
  DIAG_WAIT();
  STAMPC(63);
}

__global__ __launch_bounds__(512, 2) void fused_kernel(Params p) {
  extern __shared__ __attribute__((aligned(16))) char smem[];
  LAS char* lds = (LAS char*)smem;
  const int lane = threadIdx.x & 63;
  const int wave = __builtin_amdgcn_readfirstlane(threadIdx.x >> 6);
  const int b = blockIdx.x, chi = b & 7, sidx = b >> 3;
  Role R;
  R.i = 8 * (chi & 1) + (sidx & 7);
  R.j = 4 * (chi >> 1) + (sidx >> 3);
  R.jw = min(R.j, 13);
  R.bwork = sidx < 28;
  R.tq = R.bwork ? sidx / 7 : 0;
  R.cc = R.bwork ? sidx % 7 : 0;
  R.m64 = 4 * chi + R.tq;
  R.rho = 4 * chi + (sidx - 28);
  R.sidx = sidx;
  DIAG_INIT();
  STAMP(0);
  DIAG_WAIT();
  STAMPC(62);
  DIAG_WAIT();
  if (wave >= 4) loader_main(p, (u32)(size_t)lds, R, wave - 4, lane, wave);
  else compute_main(p, lds, R, wave, lane, wave);
  DIAG_DUMP();
}

extern "C" void kernel_launch(void* const* d_in, const int* in_sizes, int n_in, void* d_out, int out_size, void* d_ws,
                              size_t ws_size, hipStream_t stream) {
  Params p;
  p.x = (const u16*)d_in[0];
  p.wqkv = (const u16*)d_in[1];
  p.wphi = (const u16*)d_in[2];
  p.wo = (const u16*)d_in[3];
  p.w1 = (const u16*)d_in[5];
  p.b1 = (const u16*)d_in[6];
  p.w2 = (const u16*)d_in[7];
  p.b2 = (const u16*)d_in[8];
  p.out = (u16*)d_out;
  p.ws = (char*)d_ws;
  (void)hipFuncSetAttribute((const void*)fused_kernel, hipFuncAttributeMaxDynamicSharedMemorySize, LDS_BYTES);
  fused_kernel<<<256, 512, LDS_BYTES, stream>>>(p);
}
```

```cpp
#define TOUCH_POS 1
#include <hip/hip_runtime.h>
#include <hip/hip_bf16.h>
#include <stdint.h>

typedef unsigned short u16;
typedef uint32_t u32;
typedef uint64_t u64;
typedef __bf16 bf2 __attribute__((ext_vector_type(2)));
typedef __bf16 bf4 __attribute__((ext_vector_type(4)));
typedef __bf16 bf8 __attribute__((ext_vector_type(8)));
typedef float f4v __attribute__((ext_vector_type(4)));
typedef float f16v __attribute__((ext_vector_type(16)));
typedef u32 u2v __attribute__((ext_vector_type(2)));
typedef u32 u4v __attribute__((ext_vector_type(4)));
#define LAS __attribute__((address_space(3)))
#define GAS __attribute__((address_space(1)))
#define CAS __attribute__((address_space(4)))
#define DI __device__ __forceinline__

constexpr int S_ = 2048, D_ = 448, NH = 16, DH = 28, FF = 1792;
constexpr int XROW = D_ * 2;
constexpr int QROW = 3 * D_ * 2;
constexpr int W1ROW = FF * 2;

constexpr u32 WS_EPOCH = 0, WS_ERR = 128;
constexpr u32 FL0 = 4096, FLS = 128;
constexpr u32 FL_P0 = FL0;
constexpr u32 FL_C1 = FL_P0 + 768 * FLS;
constexpr u32 FL_H = FL_C1 + 1024 * FLS;
constexpr u32 FL_KV = FL_H + 1024 * FLS;
constexpr u32 FL_KR = FL_KV + 512 * FLS;
constexpr u32 FL_O = FL_KR + 128 * FLS;
constexpr u32 OFF_ST = FL_O + 1024 * FLS;
constexpr u32 OFF_WP = OFF_ST + S_ * 16;
constexpr u32 WP_HEAD = 84 * 1024, WP_Q = 56 * 1024;
constexpr u32 OFF_CSP = OFF_WP + NH * WP_HEAD;
constexpr u32 OFF_C1P = OFF_CSP + NH * 3 * 2048;
constexpr u32 H_TILE = 112 * 1024 + 128, KVP_HS = 16 * 2048 + 128, O_TILE = 32 * 1024 + 128;
constexpr u32 OFF_H = OFF_C1P + 256 * 512;
constexpr u32 OFF_KVP = OFF_H + 64 * H_TILE;
constexpr u32 OFF_KVT = OFF_KVP + NH * 2 * KVP_HS;
constexpr u32 OFF_O = OFF_KVT + NH * 4 * 1024;
constexpr u32 OFF_DIAG = OFF_O + 64 * O_TILE;
constexpr u32 WS_USED = OFF_DIAG + 256 * 8 * 64 * 8;

constexpr u32 SLOT = 16384;
constexpr u32 L_PHI = 8 * SLOT;
constexpr u32 L_V = L_PHI + 16384;
constexpr u32 L_AUG = L_V + 8192;
constexpr u32 L_DUMMY = L_AUG + 7168 + 512;
constexpr u32 L_CNT = L_DUMMY + 256;
constexpr u32 LDS_BYTES = 163840;

constexpr float LOG2E = 1.4426950408889634f;
constexpr u32 SPIN_CAP = 1u << 18;

struct Params {
  const u16 *x, *wqkv, *wphi, *wo, *w1, *b1, *w2, *b2;
  u16* out;
  char* ws;
};

#define STAMP(id)
#define STAMPC(id)
#define DIAG_WAIT()
#define DIAG_INIT()
#define DIAG_DUMP()

DI float bf_lo(u32 u) { return __uint_as_float(u << 16); }
DI float bf_hi(u32 u) { return __uint_as_float(u & 0xffff0000u); }
DI u32 pk2(float a, float b) {
  bf2 v = {(__bf16)a, (__bf16)b};
  return __builtin_bit_cast(u32, v);
}
DI u32 bf_rn(float a) { return pk2(a, 0.f) & 0xffffu; }
DI bf8 as_bf8(u4v v) { return __builtin_bit_cast(bf8, v); }
DI bf8 frag8(const f16v& a, int s) {
  u4v r = {pk2(a[8 * s], a[8 * s + 1]), pk2(a[8 * s + 2], a[8 * s + 3]), pk2(a[8 * s + 4], a[8 * s + 5]), pk2(a[8 * s + 6], a[8 * s + 7])};
  return as_bf8(r);
}
DI f16v mfma32(bf8 a, bf8 b, f16v c) { return __builtin_amdgcn_mfma_f32_32x32x16_bf16(a, b, c, 0, 0, 0); }
DI f4v mfma16(bf8 a, bf8 b, f4v c) { return __builtin_amdgcn_mfma_f32_16x16x32_bf16(a, b, c, 0, 0, 0); }
DI bf8 lds_frag(LAS const char* p) { return *(LAS const bf8*)p; }
DI bf8 lds_tr2(LAS const char* p0, LAS const char* p1) {
  bf4 a = __builtin_amdgcn_ds_read_tr16_b64_v4bf16((LAS bf4*)p0);
  bf4 b = __builtin_amdgcn_ds_read_tr16_b64_v4bf16((LAS bf4*)p1);
  return __builtin_shufflevector(a, b, 0, 1, 2, 3, 4, 5, 6, 7);
}
DI float xhalf_max(float v) {
  auto r = __builtin_amdgcn_permlane32_swap(__float_as_uint(v), __float_as_uint(v), false, false);
  return __builtin_elementwise_maximum(__uint_as_float(r[0]), __uint_as_float(r[1]));
}
DI float xhalf_min(float v) {
  auto r = __builtin_amdgcn_permlane32_swap(__float_as_uint(v), __float_as_uint(v), false, false);
  return __builtin_elementwise_minimum(__uint_as_float(r[0]), __uint_as_float(r[1]));
}
DI float xhalf_sum(float v) {
  auto r = __builtin_amdgcn_permlane32_swap(__float_as_uint(v), __float_as_uint(v), false, false);
  return __uint_as_float(r[0]) + __uint_as_float(r[1]);
}
DI float xhalf_hi(float v) {
  auto r = __builtin_amdgcn_permlane32_swap(__float_as_uint(v), __float_as_uint(v), false, false);
  return __uint_as_float(r[1]);
}
DI float dpp_ror(float v, int n) {
  const int iv = __builtin_bit_cast(int, v);
  int r;
  switch (n) {
    case 8: r = __builtin_amdgcn_update_dpp(0, iv, 0x128, 0xf, 0xf, false); break;
    case 4: r = __builtin_amdgcn_update_dpp(0, iv, 0x124, 0xf, 0xf, false); break;
    case 2: r = __builtin_amdgcn_update_dpp(0, iv, 0x122, 0xf, 0xf, false); break;
    default: r = __builtin_amdgcn_update_dpp(0, iv, 0x121, 0xf, 0xf, false); break;
  }
  return __builtin_bit_cast(float, r);
}
DI float half_sum(float v) {
  v += dpp_ror(v, 8); v += dpp_ror(v, 4); v += dpp_ror(v, 2); v += dpp_ror(v, 1);
  auto r = __builtin_amdgcn_permlane16_swap(__float_as_uint(v), __float_as_uint(v), false, false);
  return __uint_as_float(r[0]) + __uint_as_float(r[1]);
}
typedef float f2v __attribute__((ext_vector_type(2)));
DI f2v pair(const f16v& a, int r) { return f2v{a[r], a[r + 1]}; }
DI u32 pk2v(f2v v) { return pk2(v[0], v[1]); }
DI void bar() { asm volatile("s_waitcnt lgkmcnt(0)\n\ts_barrier" ::: "memory"); }
DI void bar_only() { asm volatile("s_barrier" ::: "memory"); }
template <int N> DI void vmwait() { asm volatile("s_waitcnt vmcnt(%0)" ::"i"(N) : "memory"); }
#define SCHED() __builtin_amdgcn_sched_barrier(0)

DI void dma(u32 voff, const void* sbase, u32 ldsaddr) {
  asm volatile("s_mov_b32 m0, %2\n\ts_nop 0\n\tglobal_load_lds_dwordx4 %0, %1" ::"v"(voff), "s"(sbase), "s"(ldsaddr) : "memory");
}
DI void dma_sc1(u32 voff, const void* sbase, u32 ldsaddr) {
  asm volatile("s_mov_b32 m0, %2\n\ts_nop 0\n\tglobal_load_lds_dwordx4 %0, %1 sc1" ::"v"(voff), "s"(sbase), "s"(ldsaddr) : "memory");
}

DI void dma4(u32 v0, u32 v1, u32 v2, u32 v3, const void* sbase, u32 ldsaddr) {
  asm volatile("s_mov_b32 m0, %5\n\ts_nop 0\n\tglobal_load_lds_dwordx4 %0, %4\n\tglobal_load_lds_dwordx4 %1, %4 offset:1024\n\t"
               "global_load_lds_dwordx4 %2, %4 offset:2048\n\tglobal_load_lds_dwordx4 %3, %4 offset:3072"
               ::"v"(v0), "v"(v1), "v"(v2), "v"(v3), "s"(sbase), "s"(ldsaddr) : "memory");
}
template <int N> DI void dma_lin(u32 v, const void* sbase, u32 ldsaddr) {
  if constexpr (N == 1)
    asm volatile("s_mov_b32 m0, %2\n\ts_nop 0\n\tglobal_load_lds_dwordx4 %0, %1 sc1" ::"v"(v), "s"(sbase), "s"(ldsaddr) : "memory");
  else if constexpr (N == 2)
    asm volatile("s_mov_b32 m0, %2\n\ts_nop 0\n\tglobal_load_lds_dwordx4 %0, %1 sc1\n\tglobal_load_lds_dwordx4 %0, %1 offset:1024 sc1"
                 ::"v"(v), "s"(sbase), "s"(ldsaddr) : "memory");
  else
    asm volatile("s_mov_b32 m0, %2\n\ts_nop 0\n\tglobal_load_lds_dwordx4 %0, %1 sc1\n\tglobal_load_lds_dwordx4 %0, %1 offset:1024 sc1\n\t"
                 "global_load_lds_dwordx4 %0, %1 offset:2048 sc1\n\tglobal_load_lds_dwordx4 %0, %1 offset:3072 sc1"
                 ::"v"(v), "s"(sbase), "s"(ldsaddr) : "memory");
}

typedef __amdgpu_buffer_rsrc_t rsrc_t;
DI u4v ld128(rsrc_t r, u32 voff, u32 soff) { return __builtin_amdgcn_raw_buffer_load_b128(r, voff, soff, 16); }
DI u32 ld32(rsrc_t r, u32 voff, u32 soff) { return __builtin_amdgcn_raw_buffer_load_b32(r, voff, soff, 16); }
DI void st128(rsrc_t r, u32 voff, u32 soff, u4v v) { __builtin_amdgcn_raw_buffer_store_b128(v, r, voff, soff, 16); }
DI void st64(rsrc_t r, u32 voff, u32 soff, u2v v) { __builtin_amdgcn_raw_buffer_store_b64(v, r, voff, soff, 16); }
DI void st32(rsrc_t r, u32 voff, u32 soff, u32 v) { __builtin_amdgcn_raw_buffer_store_b32(v, r, voff, soff, 16); }
DI float fmx(float a, float b) { return __builtin_elementwise_maximum(a, b); }
DI float fmn(float a, float b) { return __builtin_elementwise_minimum(a, b); }
DI void touch(u32 voff, const void* sbase, u32 lb) {
  asm volatile("s_mov_b32 m0, %2\n\ts_nop 0\n\tglobal_load_lds_dword %0, %1" ::"v"(voff), "s"(sbase), "s"(lb + L_DUMMY) : "memory");
}

DI void cnt_arrive(u32 addr, int lane) {
  asm volatile("s_waitcnt lgkmcnt(0)" ::: "memory");
  if (lane == 0) __hip_atomic_fetch_add((LAS u32*)(size_t)addr, 1u, __ATOMIC_RELAXED, __HIP_MEMORY_SCOPE_WORKGROUP);
}
DI void cnt_wait(u32 addr) {
#pragma unroll 1
  for (u32 spin = 0; spin < SPIN_CAP; ++spin)
  {
    if (__builtin_amdgcn_readfirstlane(__hip_atomic_load((LAS u32*)(size_t)addr, __ATOMIC_RELAXED, __HIP_MEMORY_SCOPE_WORKGROUP)) >= 4u) break;
    __builtin_amdgcn_s_sleep(1);
  }
  asm volatile("" ::: "memory");
}
DI u64 tag64(u32 t) { return (u64)t | ((u64)(t * 0x9E3779B1u + 0x7F4A7C15u) << 32); }

DI void flag_set(char* ws, u32 off, u64 tag, int lane) {
  asm volatile("s_waitcnt vmcnt(0)" ::: "memory");
  if (lane == 0) __hip_atomic_store((GAS u64*)(ws + off), tag, __ATOMIC_RELAXED, __HIP_MEMORY_SCOPE_AGENT);
}
DI void flag_wait(char* ws, u32 off, int n, u64 tag, int lane, u32 code) {
  GAS u64* ptr = (GAS u64*)(ws + off);
#pragma unroll 1
  for (u32 spin = 0;; ++spin) {
    u64 v = tag;
    if (lane < n) v = __hip_atomic_load(ptr, __ATOMIC_RELAXED, __HIP_MEMORY_SCOPE_AGENT);
    if (__all(v == tag)) break;
    if (spin > SPIN_CAP) {
      if (lane == 0) __hip_atomic_store((GAS u32*)(ws + WS_ERR), code, __ATOMIC_RELAXED, __HIP_MEMORY_SCOPE_AGENT);
      break;
    }
    __builtin_amdgcn_s_sleep(1);
  }
  asm volatile("" ::: "memory");
}
DI void stat_wait(char* ws, rsrc_t rs, int token, u64 tag, int lane, float& mean, float& sigma) {
#pragma unroll 1
  for (u32 spin = 0;; ++spin) {
    u4v g = ld128(rs, token * 16, OFF_ST);
    mean = __uint_as_float(g[0]);
    sigma = __uint_as_float(g[1]);
    bool ok = g[2] == (u32)tag && g[3] == (u32)(tag >> 32);
    if (__all(ok)) break;
    if (spin > SPIN_CAP) {
      if (lane == 0) __hip_atomic_store((GAS u32*)(ws + WS_ERR), 99u, __ATOMIC_RELAXED, __HIP_MEMORY_SCOPE_AGENT);
      break;
    }
    __builtin_amdgcn_s_sleep(1);
  }
}
DI void split2(float v, u32& hi, u32& lo) {
  hi = bf_rn(v);
  lo = bf_rn(v - __uint_as_float(hi << 16));
}
DI bf8 aug_w(float cs, u32 beta, int hh) {
  u32 hi, lo;
  split2(cs, hi, lo);
  u4v r = {hi | (hi << 16), lo | (beta << 16), beta, 0u};
  if (hh) r = u4v{0u, 0u, 0u, 0u};
  return as_bf8(r);
}

struct Role {
  int i, j;
  int jw;
  int m64, cc;
  int tq;
  bool bwork;
  int rho;
  int sidx;
};

DI u32 wslot(int g) { return (u32)((0x1076u >> (4 * (g & 3))) & 15) * SLOT; }
DI u32 hslot(int g) { return (u32)((0x3254u >> (4 * (g & 3))) & 15) * SLOT; }
DI constexpr u32 fslot(int c) { return (u32)(2 * (c % 4)) * SLOT; }
constexpr u32 L_KVA = 6 * SLOT;
constexpr u32 L_KVB = 0;
constexpr u32 L_Q = 2 * SLOT;
constexpr u32 L_HS = 4 * SLOT;

DI void loader_main(const Params& p, u32 lb, const Role& R, int lw, int lane, int wave) {
  const int i = R.i, j = R.j, m64 = R.m64, cc = R.cc;
  const u32 vx0 = (lane >> 3) * XROW + (((lane & 7) ^ (lane >> 4)) << 4);
  const u32 vx1 = (vx0 ^ 64) + (8 * XROW - 1024), vx2 = vx0 + 2 * (8 * XROW - 1024), vx3 = vx1 + 2 * (8 * XROW - 1024);
  const u32 vw0 = (lane >> 4) * W1ROW + (((lane & 15) ^ ((lane >> 4) << 2)) << 4);
  const u32 vw1 = vw0 + (4 * W1ROW - 1024), vw2 = vw0 + 2 * (4 * W1ROW - 1024), vw3 = vw0 + 3 * (4 * W1ROW - 1024);
  const char* xb = (const char*)p.x + (size_t)(128 * i + 32 * lw) * XROW;
  const char* w1b = (const char*)p.w1 + (size_t)(16 * lw) * W1ROW + R.jw * 256;
  const u32 lq = lb + lw * 4096;
  int gc = 0;
  auto stage = [&](u32 slot) {
    dma4(vx0, vx1, vx2, vx3, xb + gc * 128, lq + slot);
    dma4(vw0, vw1, vw2, vw3, w1b + (size_t)gc * (64 * W1ROW), lq + slot + SLOT);
    ++gc;
  };
  if (lw == 3) {
    const int t = R.sidx * 6 + min(lane, 5);
    const char* q;
    if (t < 28) q = (const char*)p.x + t * 65536;
    else if (t < 47) q = (const char*)p.wqkv + (t - 28) * 63000;
    else if (t < 48) q = (const char*)p.wphi;
    else if (t < 54) q = (const char*)p.wo + (t - 48) * 65536;
    else if (t < 79) q = (const char*)p.w1 + (t - 54) * 64000;
    else if (t < 80) q = (const char*)p.b1;
    else if (t < 105) q = (const char*)p.w2 + (t - 80) * 64000;
    else if (t < 106) q = (const char*)p.b2;
    else if (t < 134) q = (const char*)p.out + (t - 106) * 65536;
    else if (t < 166) q = p.ws + OFF_H + (size_t)(2 * (t - 134) + 1) * H_TILE - 128;
    else if (t < 174) q = p.ws + OFF_KVP + (size_t)(4 * (t - 166) + 1) * KVP_HS - 128;
    else if (t < 182) q = p.ws + OFF_O + (size_t)(8 * (t - 174) + 1) * O_TILE - 128;
    else q = p.ws + 512;
    asm volatile("s_mov_b32 m0, %1\n\ts_nop 0\n\tglobal_load_lds_dword %0, off" ::"v"(q), "s"(lb + L_DUMMY) : "memory");
  }
  auto warm = [&](int c0) {
    const int lx = min(lane, 8 * (7 - c0) - 1), lz = min(lane, 4 * (7 - c0) - 1);
    touch((32 * (j & 3) + 8 * lw + (lx & 7)) * XROW + (c0 + (lx >> 3)) * 128, (const char*)p.x + (size_t)(128 * i) * XROW, lb);
    touch((64 * (c0 + (lz >> 2)) + 8 * (i & 7) + 2 * lw + ((lz & 3) >> 1)) * W1ROW + (lz & 1) * 128, (const char*)p.w1 + R.jw * 256, lb);
  };
  bar_only();
  STAMP(12);
  stage(fslot(0));
  STAMP(13);
  warm(1);
  stage(fslot(1));
  const u32 E = *(CAS const u32*)(p.ws + WS_EPOCH);
  const u64 tag = tag64(E + 1);
  const rsrc_t rs = __builtin_amdgcn_make_buffer_rsrc(p.ws, 0, WS_USED, 0x00020000);
  const u32 vl = lane * 16;
  const u32 bchunk = ((lane & 7) ^ (((lane >> 4) & 1) << 2)) << 4;
  const u32 vb0 = (lane >> 3) * XROW + bchunk;
  const u32 vb1 = vb0 + (8 * XROW - 1024), vb2 = vb0 + 2 * (8 * XROW - 1024), vb3 = vb0 + 3 * (8 * XROW - 1024);
  const u32 vb3c = min(lane >> 3, 3) * XROW + bchunk + 3 * (8 * XROW - 1024);
  STAMP(1);
  vmwait<10>(); bar_only();
  STAMP(2);
  stage(fslot(2)); vmwait<8>(); bar_only();
  stage(fslot(3)); vmwait<8>(); bar_only();
  stage(fslot(4)); vmwait<8>(); bar_only();
  stage(fslot(5));
  const char* wp = p.ws + OFF_WP + (size_t)j * WP_HEAD;
  const u32 pflag = lane < 42 ? FL_P0 + (j * 48 + (lane / 14) * 16 + lane % 14) * FLS : FL_C1 + (R.jw * 16 + min(lane - 42, 15)) * FLS;
  dma_sc1(pflag, p.ws, lb + L_V + 4096 + lw * 1024);
  vmwait<9>(); bar_only();
  stage(fslot(6));
  vmwait<8>(); bar_only();
  STAMP(3);
  {
    const u2v f = *(LAS const u2v*)(size_t)(lb + L_V + 4096 + lw * 1024 + lane * 16);
    if (!__all(f[0] == (u32)tag && f[1] == (u32)(tag >> 32))) flag_wait(p.ws, pflag, 58, tag, lane, 1);
  }
  STAMP(4);
  const u32 gs = lb + L_PHI + lw * 4096;
  {
    const char* c1 = p.ws + OFF_C1P + (size_t)R.jw * 16 * 512 + lw * 128;
    const u32 vg = (lane >> 3) * 512 + (lane & 7) * 16;
    dma_lin<1>(vg, c1, gs);
    dma_lin<1>(vg, c1 + 8 * 512, gs + 1024);
    dma_lin<2>(vl, p.ws + OFF_CSP + (size_t)(j * 3 + min(lw, 2)) * 2048, gs + 2048);
  }
  asm volatile("s_mov_b32 m0, %2\n\ts_nop 0\n\tglobal_load_lds_dword %0, %1" ::"v"(min(lane, 15) * 4), "s"((const char*)p.b1 + (R.jw * 128 + lw * 32) * 2),
               "s"(lb + L_V + lw * 256) : "memory");
#pragma unroll
  for (int c = 0; c < 2; ++c) dma_lin<2>(vl, wp + c * 8192 + lw * 2048, lb + L_KVA + c * 8192 + lw * 2048);
  vmwait<9>(); bar_only();
  vmwait<4>();
  {
    const int hh = lane >> 5, l31 = lane & 31;
    LAS const float* src = (LAS const float*)(size_t)(gs + hh * 1024 + l31 * 4);
    float a = 0.f, b = 0.f;
#pragma unroll
    for (int q = 0; q < 8; ++q) a += src[32 * q];
    LAS const float* src2 = (LAS const float*)(size_t)(gs + 2048 + hh * 7 * 128 + l31 * 4);
#pragma unroll
    for (int q = 0; q < 7; ++q) b += src2[32 * q];
    const u32 beta1 = *(LAS const u16*)(size_t)(lb + L_V + lw * 256 + l31 * 2);
    LAS char* aug = (LAS char*)(size_t)(lb + L_AUG) + vl;
    *(LAS bf8*)(aug + lw * 1024) = aug_w(xhalf_sum(a), beta1, hh);
    if (lw < 3) *(LAS bf8*)(aug + (4 + lw) * 1024) = aug_w(xhalf_sum(b), (lw == 1 && l31 == 28) ? 0x3f80u : 0u, hh);
    asm volatile("s_waitcnt lgkmcnt(0)" ::: "memory");
  }
#pragma unroll
  for (int c = 2; c < 4; ++c) dma_lin<2>(vl, wp + c * 8192 + lw * 2048, lb + L_KVA + c * 8192 + lw * 2048);
#pragma unroll
  for (int c = 4; c < 7; ++c) dma_lin<2>(vl, wp + c * 8192 + lw * 2048, lb + L_KVB + (c - 4) * 8192 + lw * 2048);
  bar_only();
  STAMP(5);
#pragma unroll
  for (int c = 0; c < 7; ++c) dma_lin<1>(vl, wp + WP_Q + c * 4096 + lw * 1024, lb + L_Q + c * 4096 + lw * 1024);
  if (R.bwork) {
#pragma unroll 1
    for (int q = lw; q < 9; q += 4) {
      if (q < 7) touch(lane * XROW, (const char*)p.w2 + (size_t)(448 * R.tq + 64 * q) * XROW + cc * 128, lb);
      else touch(min(lane, q == 7 ? 63 : 47) * XROW, (const char*)p.wo + (size_t)(112 * R.tq + 64 * (q - 7)) * XROW + cc * 128, lb);
    }
    if (lw == 0) vmwait<10>(); else vmwait<9>();
  } else {
    vmwait<7>();
  }
  bar_only();
  if (j < 14) {
    LAS const char* src = (LAS const char*)(size_t)(lb + L_HS + lw * 8192) + vl;
    const u32 hb = OFF_H + (4 * i + lw) * H_TILE + 8 * j * 1024;
    u4v hv[8];
#pragma unroll
    for (int q = 0; q < 8; ++q) hv[q] = *(LAS const u4v*)(src + q * 1024);
#pragma unroll
    for (int q = 0; q < 8; ++q) st128(rs, vl, hb + q * 1024, hv[q]);
  }
  STAMP(14);
  vmwait<0>();
  if (j < 14 && lane == 0) __hip_atomic_store((GAS u64*)(p.ws + FL_H + ((i * 16 + j) * 4 + lw) * FLS), tag, __ATOMIC_RELAXED, __HIP_MEMORY_SCOPE_AGENT);
  cnt_arrive(lb + L_CNT + 8, lane);
  cnt_wait(lb + L_CNT + 4);
  STAMP(6);
  const char* w2p = (const char*)p.w2 + (size_t)(32 * lw) * XROW + cc * 128;
  auto issue_w = [&](int g) {
    if (g < 14) {
      dma4(vb0, vb1, vb2, vb3, w2p, lq + wslot(g));
      w2p += 128 * XROW;
    } else {
      dma4(vb0, vb1, vb2, vb3c, (const char*)p.wo + (size_t)(28 * (4 * (g - 14) + lw)) * XROW + cc * 128, lq + wslot(g));
    }
  };
  const char* hp = p.ws + OFF_H + (size_t)(2 * m64 + (lw >> 1)) * H_TILE + (lw & 1) * 4096;
  auto issue_h = [&](int g) {
    if (g == 14) hp = p.ws + OFF_O + (size_t)(2 * m64 + (lw >> 1)) * O_TILE + (lw & 1) * 4096;
    dma_lin<4>(vl, hp, lq + hslot(g));
    hp += 8192;
  };
  if (!R.bwork) return;
  issue_w(0);
  issue_w(1);
  issue_w(2);
  issue_w(3);
  flag_wait(p.ws, FL_H + (((m64 >> 1) * 16 + (lane >> 1)) * 4 + 2 * (m64 & 1) + (lane & 1)) * FLS, 28, tag, lane, 2);
  STAMP(7);
  cnt_wait(lb + L_CNT + 8);
  issue_h(0);
  touch(lane * 128, p.ws + OFF_H + (size_t)(2 * m64 + (lw >> 1)) * H_TILE + (16 * cc + 8 * (lw & 1)) * 1024, lb);
  issue_h(1);
  vmwait<5>(); bar_only();
  issue_h(2);
  vmwait<4>(); bar_only();
  issue_h(3);
  {
    const char* b = (const char*)p.x + (size_t)(64 * m64 + 16 * lw) * XROW + cc * 128;
    dma(vx0, b, lb + L_V + lw * 2048);
    dma(vx0 ^ 64, b + 8 * XROW, lb + L_V + lw * 2048 + 1024);
  }
  STAMP(8);
  vmwait<6>(); bar_only();
#pragma unroll 1
  for (int g = 2; g < 12; ++g) {
    issue_h(g + 2);
    issue_w(g + 2);
    vmwait<8>();
    bar_only();
  }
  issue_w(14);
  vmwait<4>(); bar_only();
  issue_w(15);
  dma4(vb0, vb1, vb2, vb3c, (const char*)p.wo + (size_t)(28 * (8 + lw)) * XROW + cc * 128, lb + L_PHI + lw * 4096);
  flag_wait(p.ws, FL_O + (((m64 >> 1) * 16 + (lane >> 1)) * 4 + 2 * (m64 & 1) + (lane & 1)) * FLS, 32, tag, lane, 3);
  STAMP(9);
  issue_h(14);
  issue_h(15);
  {
    const int line = min((4 * cc + lw) * 19 + min(lane, 18), 511);
    touch(line * 128 + (line >> 8) * 128, p.ws + OFF_O + (size_t)(2 * m64) * O_TILE, lb);
  }
  vmwait<5>(); bar_only();
  issue_h(16);
  issue_w(17);
  issue_h(17);
  vmwait<12>(); bar_only();
  vmwait<8>(); bar_only();
  vmwait<0>(); bar_only();
  STAMP(10);
  if (blockIdx.x == 0 && lw == 0 && lane == 0)
    __hip_atomic_store((GAS u32*)(p.ws + WS_EPOCH), E + 1, __ATOMIC_RELAXED, __HIP_MEMORY_SCOPE_AGENT);
  STAMP(11);
}

DI void exp_pair(const f16v& a, float c, f16v& ep, f16v& en) {
  float mx = fmx(a[0], a[1]), mn = fmn(a[0], a[1]);
#pragma unroll
  for (int r = 2; r < 16; r += 2) {
    mx = fmx(fmx(mx, a[r]), a[r + 1]);
    mn = fmn(fmn(mn, a[r]), a[r + 1]);
  }
  mx = xhalf_max(mx);
  mn = xhalf_min(mn);
  const f2v c2 = {c, c}, bp = {-mx * c, -mx * c}, bn = {mn * c, mn * c};
#pragma unroll
  for (int r = 0; r < 16; r += 2) {
    const f2v tp = __builtin_elementwise_fma(pair(a, r), c2, bp), tn = __builtin_elementwise_fma(pair(a, r), -c2, bn);
    ep[r] = __builtin_amdgcn_exp2f(tp[0]); ep[r + 1] = __builtin_amdgcn_exp2f(tp[1]);
    en[r] = __builtin_amdgcn_exp2f(tn[0]); en[r + 1] = __builtin_amdgcn_exp2f(tn[1]);
  }
}
DI float sum28(const f16v& e, int hh) {
  const f2v s0 = (pair(e, 0) + pair(e, 2)) + (pair(e, 4) + pair(e, 6)), s1 = pair(e, 8) + pair(e, 10), s2 = pair(e, 12) + pair(e, 14);
  const f2v t = s0 + s1;
  return xhalf_sum((t[0] + t[1]) + (hh ? 0.f : s2[0] + s2[1]));
}

DI void fold_load(const Params& p, u32 lb, int i, int head, int part, int lane, u4v (&a)[7]) {
  const int r16 = lane & 15, dg = lane >> 4;
  const int col = part == 0 ? XROW : (part == 1 ? 2 * XROW : 0);
  const char* src = (const char*)p.wqkv + (size_t)(32 * i + r16) * QROW + col + head * 56 + (dg == 3 ? 40 : dg * 16);
  a[0] = *(GAS const u4v*)src;
  a[1] = *(GAS const u4v*)(src + 16 * QROW);
  const char* wp = (const char*)p.wphi + head * 1568;
  const u32 dst = lb + L_PHI + part * 2048;
  dma(lane * 16, wp, dst);
  dma(min(lane, 33) * 16, wp + 1024, dst + 1024);
}
DI void fold_run(rsrc_t rs, LAS const char* lds, int i, int head, int part, int lane, const u4v (&a)[7]) {
  const int dg = lane >> 4, r16 = lane & 15;
  u4v bw[2];
  {
    const int q4 = r16 >> 2, pp = lane & 3;
    LAS const char* b = lds + L_PHI + part * 2048 + (8 * dg + q4) * 56 + pp * 8;
#pragma unroll
    for (int ft = 0; ft < 2; ++ft) {
      u4v v = __builtin_bit_cast(u4v, lds_tr2(b + ft * 32, b + ft * 32 + 4 * 56));
      if (dg == 3) { v[2] = 0u; v[3] = 0u; }
      if (part == 1) {
        const int t = 16 * ft + r16 - 8 * dg;
        const u32 one = (16 * ft + r16 < 28) ? ((t & 1) ? 0x3f800000u : 0x3f80u) : 0u;
#pragma unroll
        for (int q = 0; q < 4; ++q) v[q] = (t >> 1) == q ? one : 0u;
      }
      bw[ft] = v;
    }
  }
  const u32 base = OFF_WP + head * WP_HEAD + (part == 2 ? WP_Q + 2 * i * 1024 : 2 * i * 2048 + part * 1024);
  const u32 step = part == 2 ? 1024 : 2048;
  float cs = 0.f;
#pragma unroll
  for (int t = 0; t < 2; ++t) {
    u4v av = a[t];
    if (dg == 3) av = u4v{av[2], av[3], 0u, 0u};
    const f4v z = {0.f, 0.f, 0.f, 0.f};
    f4v d0 = mfma16(as_bf8(av), as_bf8(bw[0]), z), d1 = mfma16(as_bf8(av), as_bf8(bw[1]), z);
    if (part != 1) {
#pragma unroll
      for (int r = 0; r < 4; ++r) {
        const int v = __float_as_int(d1[r]);
        d1[r] = __int_as_float(__builtin_amdgcn_update_dpp(v, v, 0x15B, 0xf, 0x8, false));
      }
    }
    auto s0 = __builtin_amdgcn_permlane16_swap(pk2(d0[0], d0[1]), pk2(d1[0], d1[1]), false, false);
    auto s1 = __builtin_amdgcn_permlane16_swap(pk2(d0[2], d0[3]), pk2(d1[2], d1[3]), false, false);
    const u4v o = {s0[0], s1[0], s0[1], s1[1]};
    st128(rs, lane * 16, base + t * step, o);
    cs += ((bf_lo(o[0]) + bf_hi(o[0])) + (bf_lo(o[1]) + bf_hi(o[1]))) + ((bf_lo(o[2]) + bf_hi(o[2])) + (bf_lo(o[3]) + bf_hi(o[3])));
  }
  cs = xhalf_sum(cs);
  if (lane < 32) st32(rs, lane * 4, OFF_CSP + ((head * 3 + part) * 16 + i) * 128, __float_as_uint(cs));
}

constexpr int FD = 6, FNB = FD + 1;
template <int U>
DI void f_request(LAS const char* lds, u32 (&xa)[4], u32 (&wa)[4], bf8 (&x)[7][4], bf8 (&wf)[FNB]) {
  if constexpr (U < 112) {
    constexpr int c = U >> 4, s = (U >> 2) & 3, n = U & 3;
    if constexpr ((U & 31) == 0 && U > 0) {
#pragma unroll
      for (int q = 0; q < 4; ++q) {
        xa[q] ^= 65536u;
        wa[q] ^= 65536u;
        asm volatile("" : "+v"(xa[q]), "+v"(wa[q]));
      }
    }
    constexpr u32 xs = fslot(c) & 65535u, ws = xs + SLOT;
    if constexpr (n == 0) x[c][s] = lds_frag(lds + xs + xa[s]);
    wf[U % FNB] = lds_tr2(lds + ws + wa[n] + 4096 * s, lds + ws + wa[n] + 4096 * s + 1024);
  }
}
template <int C, int V>
DI void f_unit(LAS const char* lds, u32 (&xa)[4], u32 (&wa)[4], bf8 (&x)[7][4], bf8 (&wf)[FNB], f16v (&acc)[4]) {
  constexpr int U = 16 * C + V;
  if constexpr (V == 16 - FD && C < 6) bar_only();
  f_request<U + FD>(lds, xa, wa, x, wf);
  acc[V & 3] = mfma32(wf[U % FNB], x[C][V >> 2], acc[V & 3]);
  SCHED();
  if constexpr (V < 15) f_unit<C, V + 1>(lds, xa, wa, x, wf, acc);
}
template <int C>
DI void tick_f(LAS const char* lds, u32 (&xa)[4], u32 (&wa)[4], bf8 (&x)[7][4], bf8 (&wf)[FNB], f16v (&acc)[4]) {
  f_unit<C, 0>(lds, xa, wa, x, wf, acc);
}
template <int U>
DI void f_prime(LAS const char* lds, u32 (&xa)[4], u32 (&wa)[4], bf8 (&x)[7][4], bf8 (&wf)[FNB]) {
  f_request<U>(lds, xa, wa, x, wf);
  if constexpr (U + 1 < FD) f_prime<U + 1>(lds, xa, wa, x, wf);
}

DI void compute_main(const Params& p, LAS char* lds, const Role& R, int w, int lane, int wave) {
  const rsrc_t rs = __builtin_amdgcn_make_buffer_rsrc(p.ws, 0, WS_USED, 0x00020000);
  const int i = R.i, j = R.j;
  const int hh = lane >> 5, l31 = lane & 31;
  const bool ffn = j < 14;
  const bool fold = i < 14 && w < 3;
  const u32 E = *(CAS const u32*)(p.ws + WS_EPOCH);
  const u64 tag = tag64(E + 1);
  u32 xs7[7];
  const int stok = 128 * i + 8 * j + 2 * w + hh;
#pragma unroll
  for (int m = 0; m < 7; ++m) xs7[m] = *(GAS const u32*)((const char*)p.x + (size_t)stok * XROW + m * 128 + l31 * 4);
  auto stats = [&]() {
    f2v s2 = {0.f, 0.f};
#pragma unroll
    for (int m = 0; m < 7; ++m) s2 += f2v{bf_lo(xs7[m]), bf_hi(xs7[m])};
    const float mean = half_sum(s2[0] + s2[1]) * (1.f / D_);
    f2v q2 = {0.f, 0.f};
#pragma unroll
    for (int m = 0; m < 7; ++m) {
      const f2v a = f2v{bf_lo(xs7[m]), bf_hi(xs7[m])} - mean;
      q2 = __builtin_elementwise_fma(a, a, q2);
    }
    const float sigma = __builtin_amdgcn_sqrtf(half_sum(q2[0] + q2[1]) * (1.f / D_) + 1e-5f);
    if (l31 == 0) st128(rs, stok * 16, OFF_ST, u4v{__float_as_uint(mean), __float_as_uint(sigma), (u32)tag, (u32)(tag >> 32)});
  };
  u4v fc[7];
  if (fold) {
    fold_load(p, (u32)(size_t)lds, i, j, w, lane, fc);
  } else if (w == 3) {
    const char* src = (const char*)p.w1 + (size_t)(28 * i + (lane >> 4)) * W1ROW + R.jw * 256 + (lane & 15) * 16;
#pragma unroll
    for (int q = 0; q < 7; ++q) fc[q] = *(GAS const u4v*)(src + (size_t)q * 4 * W1ROW);
  }
  SCHED();
  bar_only();
  SCHED();
  STAMP(22);
  auto tasks = [&]() {
    __builtin_amdgcn_s_waitcnt(0x0F70);
    if (fold) {
      fold_run(rs, lds, i, j, w, lane, fc);
    } else if (w == 3) {
      float cs[8];
#pragma unroll
      for (int e = 0; e < 8; ++e) cs[e] = 0.f;
#pragma unroll
      for (int q = 0; q < 7; ++q)
#pragma unroll
        for (int e = 0; e < 4; ++e) { cs[2 * e] += bf_lo(fc[q][e]); cs[2 * e + 1] += bf_hi(fc[q][e]); }
#pragma unroll
      for (int e = 0; e < 8; ++e) {
        auto r = __builtin_amdgcn_permlane16_swap(__float_as_uint(cs[e]), __float_as_uint(cs[e]), false, false);
        cs[e] = xhalf_sum(__uint_as_float(r[0]) + __uint_as_float(r[1]));
      }
      if (ffn && lane < 32) {
        const int o = (lane >> 4) * 4;
        st128(rs, (lane & 15) * 32 + (lane >> 4) * 16, OFF_C1P + (j * 16 + i) * 512,
              u4v{__float_as_uint(o ? cs[4] : cs[0]), __float_as_uint(o ? cs[5] : cs[1]), __float_as_uint(o ? cs[6] : cs[2]), __float_as_uint(o ? cs[7] : cs[3])});
      }
    }
    stats();
  };
  u32 xa[4], wa[4];
  {
    const int row = 32 * w + l31;
    const u32 b = row * 128 + ((hh ^ ((row >> 1) & 7)) << 4);
#pragma unroll
    for (int s = 0; s < 4; ++s) xa[s] = b ^ (32 * s);
    const int q4 = (lane & 15) >> 2, pp = lane & 3, blk = (lane >> 4) & 1;
#pragma unroll
    for (int n = 0; n < 4; ++n) {
      const int nt = (n + w) & 3;
      wa[n] = (8 * hh + q4) * 256 + (4 * (nt ^ q4) + 2 * blk + (pp >> 1)) * 16 + 8 * (pp & 1);
    }
  }
  bf8 x[7][4], wf[FNB];
  f16v acc[4];
#pragma unroll
  for (int n = 0; n < 4; ++n)
#pragma unroll
    for (int r = 0; r < 16; ++r) acc[n][r] = 0.f;

  if (w == 0 && lane < 8) *(LAS volatile u32*)(lds + L_CNT + lane * 4) = 0u;
  bar();
  STAMP(2);
  f_prime<0>(lds, xa, wa, x, wf);
  tick_f<0>(lds, xa, wa, x, wf, acc);
  STAMP(30);
  tasks();
  STAMP(23);
  tick_f<1>(lds, xa, wa, x, wf, acc);
  STAMP(31);
  vmwait<1>();
  if (lane == 0) {
    if (fold) __hip_atomic_store((GAS u64*)(p.ws + FL_P0 + ((j * 3 + w) * 16 + i) * FLS), tag, __ATOMIC_RELAXED, __HIP_MEMORY_SCOPE_AGENT);
    if (ffn && w == 3) __hip_atomic_store((GAS u64*)(p.ws + FL_C1 + (j * 16 + i) * FLS), tag, __ATOMIC_RELAXED, __HIP_MEMORY_SCOPE_AGENT);
  }
  const int token = 128 * i + 32 * w + l31;
  u4v sg = ld128(rs, token * 16, OFF_ST);
  tick_f<2>(lds, xa, wa, x, wf, acc);
  STAMP(32);
  tick_f<3>(lds, xa, wa, x, wf, acc);
  STAMP(33);
  float mean = __uint_as_float(sg[0]), sigma = __uint_as_float(sg[1]);
  if (!__all(sg[2] == (u32)tag && sg[3] == (u32)(tag >> 32))) stat_wait(p.ws, rs, token, tag, lane, mean, sigma);
  tick_f<4>(lds, xa, wa, x, wf, acc);
  STAMP(34);
  bf8 xaug;
  {
    u32 mh, ml, sh, sl;
    split2(-mean, mh, ml);
    split2(sigma, sh, sl);
    u4v r = {mh | (ml << 16), mh | (sh << 16), sl, 0u};
    if (hh) r = u4v{0u, 0u, 0u, 0u};
    xaug = as_bf8(r);
  }
  const float rstd = __builtin_amdgcn_rcpf(sigma);
  tick_f<5>(lds, xa, wa, x, wf, acc);
  STAMP(35);
  tick_f<6>(lds, xa, wa, x, wf, acc);
  STAMP(4);
  bar();
  STAMP(6);
  const u32 vl = lane * 16;
  if (ffn) {
#pragma unroll
    for (int n = 0; n < 4; ++n) acc[n] = mfma32(lds_frag(lds + L_AUG + ((n + w) & 3) * 1024 + vl), xaug, acc[n]);
    LAS char* hb = lds + L_HS + w * 8192 + vl;
#pragma unroll
    for (int n = 0; n < 4; ++n) {
#pragma unroll
      for (int r = 0; r < 16; r += 2) {
        const f2v t = {fmx(acc[n][r], 0.f), fmx(acc[n][r + 1], 0.f)};
        const f2v u = t * t;
        acc[n][r] = u[0];
        acc[n][r + 1] = u[1];
      }
#pragma unroll
      for (int s = 0; s < 2; ++s) *(LAS bf8*)(hb + (2 * ((n + w) & 3) + s) * 1024) = frag8(acc[n], s);
    }
  }
  bar();
  STAMP(5);
  f16v ak, av;
#pragma unroll
  for (int r = 0; r < 16; ++r) { ak[r] = 0.f; av[r] = 0.f; }
  {
    u32 kva = vl + L_KVA;
    asm volatile("" : "+v"(kva));
    LAS const char* wa_ = lds + kva;
    LAS const char* wb_ = lds + vl + L_KVB;
    auto kvp = [&](int ks) -> LAS const char* { return ks < 16 ? wa_ + ks * 2048 : (ks < 28 ? wb_ + (ks - 16) * 2048 : wa_ + (L_AUG + 4 * 1024 - L_KVA)); };
    bf8 fk[4], fv[4];
#pragma unroll
    for (int ks = 0; ks < 3; ++ks) {
      fk[ks] = lds_frag(kvp(ks));
      fv[ks] = lds_frag(kvp(ks) + 1024);
    }
    SCHED();
#pragma unroll
    for (int ks = 0; ks < 29; ++ks) {
      if (ks + 3 < 29) {
        fk[(ks + 3) & 3] = lds_frag(kvp(ks + 3));
        fv[(ks + 3) & 3] = lds_frag(kvp(ks + 3) + 1024);
      }
      const bf8 xb = ks < 28 ? x[(ks < 28 ? ks : 0) >> 2][ks & 3] : xaug;
      ak = mfma32(fk[ks & 3], xb, ak);
      av = mfma32(fv[ks & 3], xb, av);
      __builtin_amdgcn_sched_group_barrier(0x8, 1, 0);
      __builtin_amdgcn_sched_group_barrier(0x100, 1, 0);
      __builtin_amdgcn_sched_group_barrier(0x8, 1, 0);
      __builtin_amdgcn_sched_group_barrier(0x100, 1, 0);
      SCHED();
    }
  }
  cnt_arrive((u32)(size_t)lds + L_CNT + 4, lane);
  STAMP(7);
  const float cexp = rstd * LOG2E;
  {
    f16v ep, en;
    exp_pair(ak, cexp, ep, en);
    const float ap = rstd * __builtin_amdgcn_rcpf(sum28(ep, hh)), an = rstd * __builtin_amdgcn_rcpf(sum28(en, hh));
    const int row = 32 * w + l31;
    LAS char* base = lds + row * 64 + 8 * hh;
    const int key = (l31 >> 1) & 3;
#pragma unroll
    for (int q = 0; q < 4; ++q) {
      const u32 o = (q ^ key) << 4;
      *(LAS u2v*)(base + L_PHI + o) = u2v{pk2v(pair(ep, 4 * q) * ap), pk2v(pair(ep, 4 * q + 2) * ap)};
      *(LAS u2v*)(base + L_PHI + 8192 + o) = u2v{pk2v(pair(en, 4 * q) * an), pk2v(pair(en, 4 * q + 2) * an)};
      *(LAS u2v*)(base + L_V + o) = u2v{pk2(av[4 * q], av[4 * q + 1]), pk2(av[4 * q + 2], av[4 * q + 3])};
    }
  }
  STAMP(8);
  cnt_arrive((u32)(size_t)lds + L_CNT, lane);
  if (w < 2) {
    cnt_wait((u32)(size_t)lds + L_CNT);
    STAMP(9);
    const int q4 = (lane & 15) >> 2, pp = lane & 3, blk = (lane >> 4) & 1;
    u32 ta[2];
#pragma unroll
    for (int t = 0; t < 2; ++t) {
      const int row = 8 * hh + 4 * t + q4;
      ta[t] = row * 64 + (((2 * blk + (pp >> 1)) ^ ((row >> 1) & 3)) << 4) + 8 * (pp & 1);
    }
    f16v kv;
#pragma unroll
    for (int r = 0; r < 16; ++r) kv[r] = 0.f;
    LAS const char* pa = lds + L_PHI + w * 8192;
    LAS const char* pb = lds + L_V;
#pragma unroll
    for (int ks = 0; ks < 8; ++ks)
      kv = mfma32(lds_tr2(pa + ta[0] + ks * 1024, pa + ta[1] + ks * 1024), lds_tr2(pb + ta[0] + ks * 1024, pb + ta[1] + ks * 1024), kv);
    const u32 o = OFF_KVP + (j * 2 + w) * KVP_HS + i * 2048;
    st128(rs, vl, o, __builtin_bit_cast(u4v, frag8(kv, 0)));
    st128(rs, vl, o + 1024, __builtin_bit_cast(u4v, frag8(kv, 1)));
  }
  STAMP(18);
  cnt_wait((u32)(size_t)lds + L_CNT + 8);
  f16v aq;
#pragma unroll
  for (int r = 0; r < 16; ++r) aq[r] = 0.f;
  {
    LAS const char* wb = lds + L_Q + vl;
    bf8 fq[6];
#pragma unroll
    for (int ks = 0; ks < 5; ++ks) fq[ks] = lds_frag(wb + ks * 1024);
    SCHED();
#pragma unroll
    for (int ks = 0; ks < 29; ++ks) {
      if (ks + 5 < 28) fq[(ks + 5) % 6] = lds_frag(wb + (ks + 5) * 1024);
      else if (ks + 5 == 28) fq[(ks + 5) % 6] = lds_frag(lds + L_AUG + 6 * 1024 + vl);
      aq = mfma32(fq[ks % 6], ks < 28 ? x[(ks < 28 ? ks : 0) >> 2][ks & 3] : xaug, aq);
      SCHED();
      if (ks == 15 && w < 2) flag_set(p.ws, FL_KV + ((j * 2 + w) * 16 + i) * FLS, tag, lane);
    }
  }
  STAMP(10);
  bf8 eq[2][2];
  auto qepi = [&]() {
    f16v ep, en;
    exp_pair(aq, cexp, ep, en);
    eq[0][0] = frag8(ep, 0); eq[0][1] = frag8(ep, 1);
    eq[1][0] = frag8(en, 0); eq[1][1] = frag8(en, 1);
  };
  const int m64 = R.m64, cc = R.cc;
  const int bm = w & 1, bn = w >> 1;
  u32 ha, wb;
  {
    const int q4 = (lane & 15) >> 2, pp = lane & 3, blk = (lane >> 4) & 1;
    ha = bm * 8192 + bn * 4096 + vl;
    wb = bn * 8192 + (4 * hh + q4) * 128 + (4 * (q4 >> 1) + 2 * blk + (pp >> 1)) * 16 + 8 * (pp & 1);
  }
  const u4v sg2 = ld128(rs, (64 * m64 + 32 * bm + l31) * 16, OFF_ST);
  const u32 b2 = *((const GAS u16*)p.b2 + cc * 64 + bn * 32 + l31);
  f16v a0, a1;
#pragma unroll
  for (int r = 0; r < 16; ++r) { a0[r] = 0.f; a1[r] = 0.f; }
  bf8 fh[4], f0[4], f1[4];
  u32 hp = 0, wp = 0;
  auto point = [&](int g) { hp = hslot(g) + ha; wp = (g == 16 ? L_PHI : wslot(g)) + wb; };
  auto req = [&](int ks) {
    fh[ks] = lds_frag(lds + hp + ks * 1024);
    f0[ks] = lds_tr2(lds + wp + ks * 2048, lds + wp + ks * 2048 + 1024);
    f1[ks] = lds_tr2(lds + (wp ^ 64) + ks * 2048, lds + (wp ^ 64) + ks * 2048 + 1024);
  };
  auto mm = [&](int ks) {
    a0 = mfma32(f0[ks], fh[ks], a0);
    a1 = mfma32(f1[ks], fh[ks], a1);
    __builtin_amdgcn_sched_group_barrier(0x8, 1, 0);
    __builtin_amdgcn_sched_group_barrier(0x100, 3, 0);
    __builtin_amdgcn_sched_group_barrier(0x8, 1, 0);
    __builtin_amdgcn_sched_group_barrier(0x100, 2, 0);
    SCHED();
  };
  auto tick = [&](int g) {
    req(3); mm(0);
    bar_only();
    point(g + 1);
    req(0); mm(1);
    req(1); mm(2);
    req(2); mm(3);
  };
  const u32 krflag = FL_KR + (j * 8 + min(lane, 7)) * FLS;
  int g = 0;
  u4v kt[4];
  auto oload = [&]() {
#pragma unroll
    for (int q = 0; q < 4; ++q) kt[q] = ld128(rs, vl, OFF_KVT + (j * 4 + q) * 1024);
  };
  auto ostore = [&]() {
    f16v np, nn;
#pragma unroll
    for (int r = 0; r < 16; ++r) { np[r] = 0.f; nn[r] = 0.f; }
    np = mfma32(as_bf8(kt[0]), eq[0][0], np);
    nn = mfma32(as_bf8(kt[2]), eq[1][0], nn);
    np = mfma32(as_bf8(kt[1]), eq[0][1], np);
    nn = mfma32(as_bf8(kt[3]), eq[1][1], nn);
    const float ia = __builtin_amdgcn_rcpf(xhalf_hi(np[13])), ib = __builtin_amdgcn_rcpf(xhalf_hi(nn[13]));
    const float den = xhalf_hi(np[12]) * ia + xhalf_hi(nn[12]) * ib + 1e-6f;
    const float id = __builtin_amdgcn_rcpf(den), fa = ia * id, fb = ib * id;
    u32 ov[8];
#pragma unroll
    for (int r = 0; r < 16; r += 2) ov[r >> 1] = pk2v(pair(np, r) * fa + pair(nn, r) * fb);
    if (hh) { ov[6] = 0u; ov[7] = 0u; }
    const u32 ob = OFF_O + (4 * i + w) * O_TILE + 2 * j * 1024;
    st128(rs, vl, ob, u4v{ov[0], ov[1], ov[2], ov[3]});
    st128(rs, vl, ob + 1024, u4v{ov[4], ov[5], ov[6], ov[7]});
  };
  const u32 oflag = FL_O + ((i * 16 + j) * 4 + w) * FLS;
  if (R.bwork) {
    bar();
    point(0);
    req(0); req(1); req(2);
    SCHED();
    tick(0);
    tick(1);
    g = 2;
    qepi();
    SCHED();
    u64 kr = 0;
    bool ready = false;
#pragma unroll 1
    while (g < 10) {
      tick(g);
      ++g;
      if (__all(kr == tag)) { ready = true; break; }
      if (g >= 4) kr = __hip_atomic_load((GAS u64*)(p.ws + krflag), __ATOMIC_RELAXED, __HIP_MEMORY_SCOPE_AGENT);
    }
    if (!ready) flag_wait(p.ws, krflag, 8, tag, lane, 6);
    STAMP(14);
    oload();
    tick(g); ++g;
    ostore();
    tick(g); ++g;
    flag_set(p.ws, oflag, tag, lane);
    STAMP(15);
  } else {
    const int hs = R.rho, sk = w & 1, hf = w >> 1;
    flag_wait(p.ws, FL_KV + (hs * 16 + lane) * FLS, 16, tag, lane, 5);
    STAMP(12);
    const u32 o = OFF_KVP + hs * KVP_HS + sk * 1024 + hf * 512;
    u4v t[8];
#pragma unroll
    for (int q = 0; q < 8; ++q) t[q] = ld128(rs, hh * 8 * 2048 + l31 * 16, o + q * 2048);
    f2v sm[4];
#pragma unroll
    for (int e = 0; e < 4; ++e) sm[e] = f2v{bf_lo(t[0][e]), bf_hi(t[0][e])};
#pragma unroll
    for (int q = 1; q < 8; ++q)
#pragma unroll
      for (int e = 0; e < 4; ++e) sm[e] += f2v{bf_lo(t[q][e]), bf_hi(t[q][e])};
    u4v r;
#pragma unroll
    for (int e = 0; e < 4; ++e) r[e] = pk2(xhalf_sum(sm[e][0]), xhalf_sum(sm[e][1]));
    if (l31 == 29) r = u4v{0x3f803f80u, 0x3f803f80u, 0x3f803f80u, 0x3f803f80u};
    if (l31 >= 30) r = u4v{0u, 0u, 0u, 0u};
    if (sk == 1 && hf == 1) { r[2] = 0u; r[3] = 0u; }
    if (lane < 32) st128(rs, l31 * 16, OFF_KVT + (hs * 2 + sk) * 1024 + hf * 512, r);
    flag_set(p.ws, FL_KR + ((hs * 2 + sk) * 2 + hf) * FLS, tag, lane);
    STAMP(13);
    qepi();
    flag_wait(p.ws, krflag, 8, tag, lane, 6);
    STAMP(14);
    oload();
    ostore();
    flag_set(p.ws, oflag, tag, lane);
    STAMP(15);
    return;
  }
#pragma unroll 1
  for (; g < 13; ++g) tick(g);
  req(3); mm(0); mm(1); mm(2); mm(3);
  STAMP(24);
  float sil[16];
  u2v xr[4];
  {
    const u32 cb = (u32)(size_t)lds + L_CNT;
    cnt_arrive(cb + 12, lane);
    cnt_wait(cb + 12);
    LAS char* dst = lds + 6 * SLOT + (bm * 2 + (1 - bn)) * 4096 + vl;
    LAS const char* src = lds + 6 * SLOT + (bm * 2 + bn) * 4096 + vl;
#pragma unroll
    for (int q = 0; q < 4; ++q)
      *(LAS f4v*)(dst + q * 1024) = bn ? f4v{a0[4 * q], a0[4 * q + 1], a0[4 * q + 2], a0[4 * q + 3]} : f4v{a1[4 * q], a1[4 * q + 1], a1[4 * q + 2], a1[4 * q + 3]};
    cnt_arrive(cb + 16, lane);
    float m2 = __uint_as_float(sg2[0]), s2 = __uint_as_float(sg2[1]);
    if (!__all(sg2[2] == (u32)tag && sg2[3] == (u32)(tag >> 32))) stat_wait(p.ws, rs, 64 * m64 + 32 * bm + l31, tag, lane, m2, s2);
    const float var = s2 * s2;
    u32 vh, vlo;
    split2(var, vh, vlo);
    u4v xa2 = {vh | (vlo << 16), 0u, 0u, 0u};
    u4v wa3 = {b2 | (b2 << 16), 0u, 0u, 0u};
    if (hh) { xa2[0] = 0u; wa3[0] = 0u; }
    f16v ag = bn ? a1 : a0;
    ag = mfma32(as_bf8(wa3), as_bf8(xa2), ag);
    const float r2 = __builtin_amdgcn_rcpf(var);
    {
      const int row = 32 * bm + l31, key = (row >> 1) & 7;
      LAS const char* base = lds + L_V + row * 128 + 8 * hh;
#pragma unroll
      for (int q = 0; q < 4; ++q) xr[q] = *(LAS const u2v*)(base + (((4 * bn + q) ^ key) << 4));
    }
    cnt_wait(cb + 16);
#pragma unroll
    for (int q = 0; q < 4; ++q) {
      const f4v gv = *(LAS const f4v*)(src + q * 1024);
#pragma unroll
      for (int e = 0; e < 4; ++e) {
        const float gp = (ag[4 * q + e] + gv[e]) * r2;
        sil[4 * q + e] = gp * __builtin_amdgcn_rcpf(1.f + __builtin_amdgcn_exp2f(-gp * LOG2E));
      }
    }
  }
  f16v as;
#pragma unroll
  for (int r = 0; r < 16; ++r) as[r] = 0.f;
  {
    const int q4 = (lane & 15) >> 2, pp = lane & 3, blk = (lane >> 4) & 1;
    const u32 hs = bm * 8192 + vl, ws = (4 * hh + q4) * 128 + (4 * (bn ^ (q4 >> 1)) + 2 * blk + (pp >> 1)) * 16 + 8 * (pp & 1);
    bf8 sh[2][8], sw[2][8];
    auto rd = [&](int set, u32 hslot_, u32 wslot_) {
      LAS const char* hp_ = lds + hslot_ + hs;
      LAS const char* wp_ = lds + wslot_ + ws;
#pragma unroll
      for (int ks = 0; ks < 8; ++ks) {
        sh[set][ks] = lds_frag(hp_ + ks * 1024);
        sw[set][ks] = lds_tr2(wp_ + ks * 2048, wp_ + ks * 2048 + 1024);
      }
    };
    auto ms = [&](int set) {
#pragma unroll
      for (int ks = 0; ks < 8; ++ks) as = mfma32(sw[set][ks], sh[set][ks], as);
    };
    auto mix = [&]() {
#pragma unroll
      for (int ks = 0; ks < 8; ++ks) {
        __builtin_amdgcn_sched_group_barrier(0x8, 1, 0);
        __builtin_amdgcn_sched_group_barrier(0x100, 3, 0);
      }
      SCHED();
    };
    bar_only();
    rd(0, 2 * SLOT, 0 * SLOT);
    SCHED();
    bar_only();
    rd(1, 3 * SLOT, 1 * SLOT); ms(0); mix();
    bar_only();
    rd(0, 4 * SLOT, L_PHI); ms(1); mix();
    bar_only();
    rd(1, 5 * SLOT, 7 * SLOT); ms(0); mix();
    ms(1);
  }
  STAMP(16);
  {
    u32 ov[4][2];
#pragma unroll
    for (int q = 0; q < 4; ++q) {
      const float o0 = __builtin_fmaf(as[4 * q], sil[4 * q], bf_lo(xr[q][0])), o1 = __builtin_fmaf(as[4 * q + 1], sil[4 * q + 1], bf_hi(xr[q][0]));
      const float o2 = __builtin_fmaf(as[4 * q + 2], sil[4 * q + 2], bf_lo(xr[q][1])), o3 = __builtin_fmaf(as[4 * q + 3], sil[4 * q + 3], bf_hi(xr[q][1]));
      ov[q][0] = pk2(o0, o1);
      ov[q][1] = pk2(o2, o3);
    }
    char* orow = (char*)p.out + (size_t)(64 * m64 + 32 * bm + l31) * XROW + cc * 128 + bn * 64 + hh * 16;
#pragma unroll
    for (int q = 0; q < 4; q += 2) {
      auto r0 = __builtin_amdgcn_permlane32_swap(ov[q][0], ov[q + 1][0], false, false);
      auto r1 = __builtin_amdgcn_permlane32_swap(ov[q][1], ov[q + 1][1], false, false);
      *(GAS u4v*)(orow + q * 16) = u4v{r0[0], r1[0], r0[1], r1[1]};
    }
  }
  STAMP(17);
  DIAG_WAIT();
  STAMPC(63);
}

__global__ __launch_bounds__(512, 2) void fused_kernel(Params p) {
  extern __shared__ __attribute__((aligned(16))) char smem[];
  LAS char* lds = (LAS char*)smem;
  const int lane = threadIdx.x & 63;
  const int wave = __builtin_amdgcn_readfirstlane(threadIdx.x >> 6);
  const int b = blockIdx.x, chi = b & 7, sidx = b >> 3;
  Role R;
  R.i = 8 * (chi & 1) + (sidx & 7);
  R.j = 4 * (chi >> 1) + (sidx >> 3);
  R.jw = min(R.j, 13);
  R.bwork = sidx < 28;
  R.tq = R.bwork ? sidx / 7 : 0;
  R.cc = R.bwork ? sidx % 7 : 0;
  R.m64 = 4 * chi + R.tq;
  R.rho = 4 * chi + (sidx - 28);
  R.sidx = sidx;
  DIAG_INIT();
  STAMP(0);
  DIAG_WAIT();
  STAMPC(62);
  DIAG_WAIT();
  if (wave >= 4) loader_main(p, (u32)(size_t)lds, R, wave - 4, lane, wave);
  else compute_main(p, lds, R, wave, lane, wave);
  DIAG_DUMP();
}

extern "C" void kernel_launch(void* const* d_in, const int* in_sizes, int n_in, void* d_out, int out_size, void* d_ws,
                              size_t ws_size, hipStream_t stream) {
  Params p;
  p.x = (const u16*)d_in[0];
  p.wqkv = (const u16*)d_in[1];
  p.wphi = (const u16*)d_in[2];
  p.wo = (const u16*)d_in[3];
  p.w1 = (const u16*)d_in[5];
  p.b1 = (const u16*)d_in[6];
  p.w2 = (const u16*)d_in[7];
  p.b2 = (const u16*)d_in[8];
  p.out = (u16*)d_out;
  p.ws = (char*)d_ws;
  (void)hipFuncSetAttribute((const void*)fused_kernel, hipFuncAttributeMaxDynamicSharedMemorySize, LDS_BYTES);
  fused_kernel<<<256, 512, LDS_BYTES, stream>>>(p);
}
```

```cpp
#define TOUCH_POS 1
#include <hip/hip_runtime.h>
#include <hip/hip_bf16.h>
#include <stdint.h>

typedef unsigned short u16;
typedef uint32_t u32;
typedef uint64_t u64;
typedef __bf16 bf2 __attribute__((ext_vector_type(2)));
typedef __bf16 bf4 __attribute__((ext_vector_type(4)));
typedef __bf16 bf8 __attribute__((ext_vector_type(8)));
typedef float f4v __attribute__((ext_vector_type(4)));
typedef float f16v __attribute__((ext_vector_type(16)));
typedef u32 u2v __attribute__((ext_vector_type(2)));
typedef u32 u4v __attribute__((ext_vector_type(4)));
#define LAS __attribute__((address_space(3)))
#define GAS __attribute__((address_space(1)))
#define CAS __attribute__((address_space(4)))
#define DI __device__ __forceinline__

constexpr int S_ = 2048, D_ = 448, NH = 16, DH = 28, FF = 1792;
constexpr int XROW = D_ * 2;
constexpr int QROW = 3 * D_ * 2;
constexpr int W1ROW = FF * 2;

constexpr u32 WS_EPOCH = 0, WS_ERR = 128;
constexpr u32 FL0 = 4096, FLS = 128;
constexpr u32 FL_P0 = FL0;
constexpr u32 FL_C1 = FL_P0 + 768 * FLS;
constexpr u32 FL_H = FL_C1 + 1024 * FLS;
constexpr u32 FL_KV = FL_H + 1024 * FLS;
constexpr u32 FL_KR = FL_KV + 512 * FLS;
constexpr u32 FL_O = FL_KR + 128 * FLS;
constexpr u32 OFF_ST = FL_O + 1024 * FLS;
constexpr u32 OFF_WP = OFF_ST + S_ * 16;
constexpr u32 WP_HEAD = 84 * 1024, WP_Q = 56 * 1024;
constexpr u32 OFF_CSP = OFF_WP + NH * WP_HEAD;
constexpr u32 OFF_C1P = OFF_CSP + NH * 3 * 2048;
constexpr u32 H_TILE = 112 * 1024 + 128, KVP_HS = 16 * 2048 + 128, O_TILE = 32 * 1024 + 128;
constexpr u32 OFF_H = OFF_C1P + 256 * 512;
constexpr u32 OFF_KVP = OFF_H + 64 * H_TILE;
constexpr u32 OFF_KVT = OFF_KVP + NH * 2 * KVP_HS;
constexpr u32 OFF_O = OFF_KVT + NH * 4 * 1024;
constexpr u32 OFF_DIAG = OFF_O + 64 * O_TILE;
constexpr u32 WS_USED = OFF_DIAG + 256 * 8 * 64 * 8;

constexpr u32 SLOT = 16384;
constexpr u32 L_PHI = 8 * SLOT;
constexpr u32 L_V = L_PHI + 16384;
constexpr u32 L_AUG = L_V + 8192;
constexpr u32 L_DUMMY = L_AUG + 7168 + 512;
constexpr u32 L_CNT = L_DUMMY + 256;
constexpr u32 LDS_BYTES = 163840;

constexpr float LOG2E = 1.4426950408889634f;
constexpr u32 SPIN_CAP = 1u << 18;

struct Params {
  const u16 *x, *wqkv, *wphi, *wo, *w1, *b1, *w2, *b2;
  u16* out;
  char* ws;
};

#define STAMP(id)
#define STAMPC(id)
#define DIAG_WAIT()
#define DIAG_INIT()
#define DIAG_DUMP()

DI float bf_lo(u32 u) { return __uint_as_float(u << 16); }
DI float bf_hi(u32 u) { return __uint_as_float(u & 0xffff0000u); }
DI u32 pk2(float a, float b) {
  bf2 v = {(__bf16)a, (__bf16)b};
  return __builtin_bit_cast(u32, v);
}
DI u32 bf_rn(float a) { return pk2(a, 0.f) & 0xffffu; }
DI bf8 as_bf8(u4v v) { return __builtin_bit_cast(bf8, v); }
DI bf8 frag8(const f16v& a, int s) {
  u4v r = {pk2(a[8 * s], a[8 * s + 1]), pk2(a[8 * s + 2], a[8 * s + 3]), pk2(a[8 * s + 4], a[8 * s + 5]), pk2(a[8 * s + 6], a[8 * s + 7])};
  return as_bf8(r);
}
DI f16v mfma32(bf8 a, bf8 b, f16v c) { return __builtin_amdgcn_mfma_f32_32x32x16_bf16(a, b, c, 0, 0, 0); }
DI f4v mfma16(bf8 a, bf8 b, f4v c) { return __builtin_amdgcn_mfma_f32_16x16x32_bf16(a, b, c, 0, 0, 0); }
DI bf8 lds_frag(LAS const char* p) { return *(LAS const bf8*)p; }
DI bf8 lds_tr2(LAS const char* p0, LAS const char* p1) {
  bf4 a = __builtin_amdgcn_ds_read_tr16_b64_v4bf16((LAS bf4*)p0);
  bf4 b = __builtin_amdgcn_ds_read_tr16_b64_v4bf16((LAS bf4*)p1);
  return __builtin_shufflevector(a, b, 0, 1, 2, 3, 4, 5, 6, 7);
}
DI float xhalf_max(float v) {
  auto r = __builtin_amdgcn_permlane32_swap(__float_as_uint(v), __float_as_uint(v), false, false);
  return __builtin_elementwise_maximum(__uint_as_float(r[0]), __uint_as_float(r[1]));
}
DI float xhalf_min(float v) {
  auto r = __builtin_amdgcn_permlane32_swap(__float_as_uint(v), __float_as_uint(v), false, false);
  return __builtin_elementwise_minimum(__uint_as_float(r[0]), __uint_as_float(r[1]));
}
DI float xhalf_sum(float v) {
  auto r = __builtin_amdgcn_permlane32_swap(__float_as_uint(v), __float_as_uint(v), false, false);
  return __uint_as_float(r[0]) + __uint_as_float(r[1]);
}
DI float xhalf_hi(float v) {
  auto r = __builtin_amdgcn_permlane32_swap(__float_as_uint(v), __float_as_uint(v), false, false);
  return __uint_as_float(r[1]);
}
DI float dpp_ror(float v, int n) {
  const int iv = __builtin_bit_cast(int, v);
  int r;
  switch (n) {
    case 8: r = __builtin_amdgcn_update_dpp(0, iv, 0x128, 0xf, 0xf, false); break;
    case 4: r = __builtin_amdgcn_update_dpp(0, iv, 0x124, 0xf, 0xf, false); break;
    case 2: r = __builtin_amdgcn_update_dpp(0, iv, 0x122, 0xf, 0xf, false); break;
    default: r = __builtin_amdgcn_update_dpp(0, iv, 0x121, 0xf, 0xf, false); break;
  }
  return __builtin_bit_cast(float, r);
}
DI float half_sum(float v) {
  v += dpp_ror(v, 8); v += dpp_ror(v, 4); v += dpp_ror(v, 2); v += dpp_ror(v, 1);
  auto r = __builtin_amdgcn_permlane16_swap(__float_as_uint(v), __float_as_uint(v), false, false);
  return __uint_as_float(r[0]) + __uint_as_float(r[1]);
}
typedef float f2v __attribute__((ext_vector_type(2)));
DI f2v pair(const f16v& a, int r) { return f2v{a[r], a[r + 1]}; }
DI u32 pk2v(f2v v) { return pk2(v[0], v[1]); }
DI void bar() { asm volatile("s_waitcnt lgkmcnt(0)\n\ts_barrier" ::: "memory"); }
DI void bar_only() { asm volatile("s_barrier" ::: "memory"); }
template <int N> DI void vmwait() { asm volatile("s_waitcnt vmcnt(%0)" ::"i"(N) : "memory"); }
#define SCHED() __builtin_amdgcn_sched_barrier(0)

DI void dma(u32 voff, const void* sbase, u32 ldsaddr) {
  asm volatile("s_mov_b32 m0, %2\n\ts_nop 0\n\tglobal_load_lds_dwordx4 %0, %1" ::"v"(voff), "s"(sbase), "s"(ldsaddr) : "memory");
}
DI void dma_sc1(u32 voff, const void* sbase, u32 ldsaddr) {
  asm volatile("s_mov_b32 m0, %2\n\ts_nop 0\n\tglobal_load_lds_dwordx4 %0, %1 sc1" ::"v"(voff), "s"(sbase), "s"(ldsaddr) : "memory");
}

DI void dma4(u32 v0, u32 v1, u32 v2, u32 v3, const void* sbase, u32 ldsaddr) {
  asm volatile("s_mov_b32 m0, %5\n\ts_nop 0\n\tglobal_load_lds_dwordx4 %0, %4\n\tglobal_load_lds_dwordx4 %1, %4 offset:1024\n\t"
               "global_load_lds_dwordx4 %2, %4 offset:2048\n\tglobal_load_lds_dwordx4 %3, %4 offset:3072"
               ::"v"(v0), "v"(v1), "v"(v2), "v"(v3), "s"(sbase), "s"(ldsaddr) : "memory");
}
template <int N> DI void dma_lin(u32 v, const void* sbase, u32 ldsaddr) {
  if constexpr (N == 1)
    asm volatile("s_mov_b32 m0, %2\n\ts_nop 0\n\tglobal_load_lds_dwordx4 %0, %1 sc1" ::"v"(v), "s"(sbase), "s"(ldsaddr) : "memory");
  else if constexpr (N == 2)
    asm volatile("s_mov_b32 m0, %2\n\ts_nop 0\n\tglobal_load_lds_dwordx4 %0, %1 sc1\n\tglobal_load_lds_dwordx4 %0, %1 offset:1024 sc1"
                 ::"v"(v), "s"(sbase), "s"(ldsaddr) : "memory");
  else
    asm volatile("s_mov_b32 m0, %2\n\ts_nop 0\n\tglobal_load_lds_dwordx4 %0, %1 sc1\n\tglobal_load_lds_dwordx4 %0, %1 offset:1024 sc1\n\t"
                 "global_load_lds_dwordx4 %0, %1 offset:2048 sc1\n\tglobal_load_lds_dwordx4 %0, %1 offset:3072 sc1"
                 ::"v"(v), "s"(sbase), "s"(ldsaddr) : "memory");
}

typedef __amdgpu_buffer_rsrc_t rsrc_t;
DI u4v ld128(rsrc_t r, u32 voff, u32 soff) { return __builtin_amdgcn_raw_buffer_load_b128(r, voff, soff, 16); }
DI u32 ld32(rsrc_t r, u32 voff, u32 soff) { return __builtin_amdgcn_raw_buffer_load_b32(r, voff, soff, 16); }
DI void st128(rsrc_t r, u32 voff, u32 soff, u4v v) { __builtin_amdgcn_raw_buffer_store_b128(v, r, voff, soff, 16); }
DI void st64(rsrc_t r, u32 voff, u32 soff, u2v v) { __builtin_amdgcn_raw_buffer_store_b64(v, r, voff, soff, 16); }
DI void st32(rsrc_t r, u32 voff, u32 soff, u32 v) { __builtin_amdgcn_raw_buffer_store_b32(v, r, voff, soff, 16); }
DI float fmx(float a, float b) { return __builtin_elementwise_maximum(a, b); }
DI float fmn(float a, float b) { return __builtin_elementwise_minimum(a, b); }
DI void touch(u32 voff, const void* sbase, u32 lb) {
  asm volatile("s_mov_b32 m0, %2\n\ts_nop 0\n\tglobal_load_lds_dword %0, %1" ::"v"(voff), "s"(sbase), "s"(lb + L_DUMMY) : "memory");
}

DI void cnt_arrive(u32 addr, int lane) {
  asm volatile("s_waitcnt lgkmcnt(0)" ::: "memory");
  if (lane == 0) __hip_atomic_fetch_add((LAS u32*)(size_t)addr, 1u, __ATOMIC_RELAXED, __HIP_MEMORY_SCOPE_WORKGROUP);
}
DI void cnt_wait(u32 addr) {
#pragma unroll 1
  for (u32 spin = 0; spin < SPIN_CAP; ++spin)
  {
    if (__builtin_amdgcn_readfirstlane(__hip_atomic_load((LAS u32*)(size_t)addr, __ATOMIC_RELAXED, __HIP_MEMORY_SCOPE_WORKGROUP)) >= 4u) break;
    __builtin_amdgcn_s_sleep(1);
  }
  asm volatile("" ::: "memory");
}
DI u64 tag64(u32 t) { return (u64)t | ((u64)(t * 0x9E3779B1u + 0x7F4A7C15u) << 32); }

DI void flag_set(char* ws, u32 off, u64 tag, int lane) {
  asm volatile("s_waitcnt vmcnt(0)" ::: "memory");
  if (lane == 0) __hip_atomic_store((GAS u64*)(ws + off), tag, __ATOMIC_RELAXED, __HIP_MEMORY_SCOPE_AGENT);
}
DI void flag_wait(char* ws, u32 off, int n, u64 tag, int lane, u32 code) {
  GAS u64* ptr = (GAS u64*)(ws + off);
#pragma unroll 1
  for (u32 spin = 0;; ++spin) {
    u64 v = tag;
    if (lane < n) v = __hip_atomic_load(ptr, __ATOMIC_RELAXED, __HIP_MEMORY_SCOPE_AGENT);
    if (__all(v == tag)) break;
    if (spin > SPIN_CAP) {
      if (lane == 0) __hip_atomic_store((GAS u32*)(ws + WS_ERR), code, __ATOMIC_RELAXED, __HIP_MEMORY_SCOPE_AGENT);
      break;
    }
    __builtin_amdgcn_s_sleep(1);
  }
  asm volatile("" ::: "memory");
}
DI void stat_wait(char* ws, rsrc_t rs, int token, u64 tag, int lane, float& mean, float& sigma) {
#pragma unroll 1
  for (u32 spin = 0;; ++spin) {
    u4v g = ld128(rs, token * 16, OFF_ST);
    mean = __uint_as_float(g[0]);
    sigma = __uint_as_float(g[1]);
    bool ok = g[2] == (u32)tag && g[3] == (u32)(tag >> 32);
    if (__all(ok)) break;
    if (spin > SPIN_CAP) {
      if (lane == 0) __hip_atomic_store((GAS u32*)(ws + WS_ERR), 99u, __ATOMIC_RELAXED, __HIP_MEMORY_SCOPE_AGENT);
      break;
    }
    __builtin_amdgcn_s_sleep(1);
  }
}
DI void split2(float v, u32& hi, u32& lo) {
  hi = bf_rn(v);
  lo = bf_rn(v - __uint_as_float(hi << 16));
}
DI bf8 aug_w(float cs, u32 beta, int hh) {
  u32 hi, lo;
  split2(cs, hi, lo);
  u4v r = {hi | (hi << 16), lo | (beta << 16), beta, 0u};
  if (hh) r = u4v{0u, 0u, 0u, 0u};
  return as_bf8(r);
}

struct Role {
  int i, j;
  int jw;
  int m64, cc;
  int tq;
  bool bwork;
  int rho;
  int sidx;
};

DI u32 wslot(int g) { return (u32)((0x1076u >> (4 * (g & 3))) & 15) * SLOT; }
DI u32 hslot(int g) { return (u32)((0x3254u >> (4 * (g & 3))) & 15) * SLOT; }
DI constexpr u32 fslot(int c) { return (u32)(2 * (c % 4)) * SLOT; }
constexpr u32 L_KVA = 6 * SLOT;
constexpr u32 L_KVB = 0;
constexpr u32 L_Q = 2 * SLOT;
constexpr u32 L_HS = 4 * SLOT;

DI void loader_main(const Params& p, u32 lb, const Role& R, int lw, int lane, int wave) {
  const int i = R.i, j = R.j, m64 = R.m64, cc = R.cc;
  const u32 vx0 = (lane >> 3) * XROW + (((lane & 7) ^ (lane >> 4)) << 4);
  const u32 vx1 = (vx0 ^ 64) + (8 * XROW - 1024), vx2 = vx0 + 2 * (8 * XROW - 1024), vx3 = vx1 + 2 * (8 * XROW - 1024);
  const u32 vw0 = (lane >> 4) * W1ROW + (((lane & 15) ^ ((lane >> 4) << 2)) << 4);
  const u32 vw1 = vw0 + (4 * W1ROW - 1024), vw2 = vw0 + 2 * (4 * W1ROW - 1024), vw3 = vw0 + 3 * (4 * W1ROW - 1024);
  const char* xb = (const char*)p.x + (size_t)(128 * i + 32 * lw) * XROW;
  const char* w1b = (const char*)p.w1 + (size_t)(16 * lw) * W1ROW + R.jw * 256;
  const u32 lq = lb + lw * 4096;
  int gc = 0;
  auto stage = [&](u32 slot) {
    dma4(vx0, vx1, vx2, vx3, xb + gc * 128, lq + slot);
    dma4(vw0, vw1, vw2, vw3, w1b + (size_t)gc * (64 * W1ROW), lq + slot + SLOT);
    ++gc;
  };
  if (lw == 3) {
    const int t = R.sidx * 6 + min(lane, 5);
    const char* q;
    if (t < 28) q = (const char*)p.x + t * 65536;
    else if (t < 47) q = (const char*)p.wqkv + (t - 28) * 63000;
    else if (t < 48) q = (const char*)p.wphi;
    else if (t < 54) q = (const char*)p.wo + (t - 48) * 65536;
    else if (t < 79) q = (const char*)p.w1 + (t - 54) * 64000;
    else if (t < 80) q = (const char*)p.b1;
    else if (t < 105) q = (const char*)p.w2 + (t - 80) * 64000;
    else if (t < 106) q = (const char*)p.b2;
    else if (t < 134) q = (const char*)p.out + (t - 106) * 65536;
    else if (t < 166) q = p.ws + OFF_H + (size_t)(2 * (t - 134) + 1) * H_TILE - 128;
    else if (t < 174) q = p.ws + OFF_KVP + (size_t)(4 * (t - 166) + 1) * KVP_HS - 128;
    else if (t < 182) q = p.ws + OFF_O + (size_t)(8 * (t - 174) + 1) * O_TILE - 128;
    else q = p.ws + 512;
    asm volatile("s_mov_b32 m0, %1\n\ts_nop 0\n\tglobal_load_lds_dword %0, off" ::"v"(q), "s"(lb + L_DUMMY) : "memory");
  }
  auto warm = [&](int c0) {
    const int lx = min(lane, 8 * (7 - c0) - 1), lz = min(lane, 4 * (7 - c0) - 1);
    touch((32 * (j & 3) + 8 * lw + (lx & 7)) * XROW + (c0 + (lx >> 3)) * 128, (const char*)p.x + (size_t)(128 * i) * XROW, lb);
    touch((64 * (c0 + (lz >> 2)) + 8 * (i & 7) + 2 * lw + ((lz & 3) >> 1)) * W1ROW + (lz & 1) * 128, (const char*)p.w1 + R.jw * 256, lb);
  };
  bar_only();
  STAMP(12);
  stage(fslot(0));
  STAMP(13);
  warm(1);
  stage(fslot(1));
  const u32 E = *(CAS const u32*)(p.ws + WS_EPOCH);
  const u64 tag = tag64(E + 1);
  const rsrc_t rs = __builtin_amdgcn_make_buffer_rsrc(p.ws, 0, WS_USED, 0x00020000);
  const u32 vl = lane * 16;
  const u32 bchunk = ((lane & 7) ^ (((lane >> 4) & 1) << 2)) << 4;
  const u32 vb0 = (lane >> 3) * XROW + bchunk;
  const u32 vb1 = vb0 + (8 * XROW - 1024), vb2 = vb0 + 2 * (8 * XROW - 1024), vb3 = vb0 + 3 * (8 * XROW - 1024);
  const u32 vb3c = min(lane >> 3, 3) * XROW + bchunk + 3 * (8 * XROW - 1024);
  STAMP(1);
  vmwait<10>(); bar_only();
  STAMP(2);
  stage(fslot(2));
  stage(fslot(3));
  vmwait<16>(); bar_only();
  vmwait<8>(); bar_only();
  stage(fslot(4)); vmwait<8>(); bar_only();
  stage(fslot(5));
  const char* wp = p.ws + OFF_WP + (size_t)j * WP_HEAD;
  const u32 pflag = lane < 42 ? FL_P0 + (j * 48 + (lane / 14) * 16 + lane % 14) * FLS : FL_C1 + (R.jw * 16 + min(lane - 42, 15)) * FLS;
  dma_sc1(pflag, p.ws, lb + L_V + 4096 + lw * 1024);
  vmwait<9>(); bar_only();
  stage(fslot(6));
  vmwait<8>(); bar_only();
  STAMP(3);
  {
    const u2v f = *(LAS const u2v*)(size_t)(lb + L_V + 4096 + lw * 1024 + lane * 16);
    if (!__all(f[0] == (u32)tag && f[1] == (u32)(tag >> 32))) flag_wait(p.ws, pflag, 58, tag, lane, 1);
  }
  STAMP(4);
  const u32 gs = lb + L_PHI + lw * 4096;
  {
    const char* c1 = p.ws + OFF_C1P + (size_t)R.jw * 16 * 512 + lw * 128;
    const u32 vg = (lane >> 3) * 512 + (lane & 7) * 16;
    dma_lin<1>(vg, c1, gs);
    dma_lin<1>(vg, c1 + 8 * 512, gs + 1024);
    dma_lin<2>(vl, p.ws + OFF_CSP + (size_t)(j * 3 + min(lw, 2)) * 2048, gs + 2048);
  }
  asm volatile("s_mov_b32 m0, %2\n\ts_nop 0\n\tglobal_load_lds_dword %0, %1" ::"v"(min(lane, 15) * 4), "s"((const char*)p.b1 + (R.jw * 128 + lw * 32) * 2),
               "s"(lb + L_V + lw * 256) : "memory");
#pragma unroll
  for (int c = 0; c < 2; ++c) dma_lin<2>(vl, wp + c * 8192 + lw * 2048, lb + L_KVA + c * 8192 + lw * 2048);
  vmwait<9>(); bar_only();
  vmwait<4>();
  {
    const int hh = lane >> 5, l31 = lane & 31;
    LAS const float* src = (LAS const float*)(size_t)(gs + hh * 1024 + l31 * 4);
    float a = 0.f, b = 0.f;
#pragma unroll
    for (int q = 0; q < 8; ++q) a += src[32 * q];
    LAS const float* src2 = (LAS const float*)(size_t)(gs + 2048 + hh * 7 * 128 + l31 * 4);
#pragma unroll
    for (int q = 0; q < 7; ++q) b += src2[32 * q];
    const u32 beta1 = *(LAS const u16*)(size_t)(lb + L_V + lw * 256 + l31 * 2);
    LAS char* aug = (LAS char*)(size_t)(lb + L_AUG) + vl;
    *(LAS bf8*)(aug + lw * 1024) = aug_w(xhalf_sum(a), beta1, hh);
    if (lw < 3) *(LAS bf8*)(aug + (4 + lw) * 1024) = aug_w(xhalf_sum(b), (lw == 1 && l31 == 28) ? 0x3f80u : 0u, hh);
    asm volatile("s_waitcnt lgkmcnt(0)" ::: "memory");
  }
#pragma unroll
  for (int c = 2; c < 4; ++c) dma_lin<2>(vl, wp + c * 8192 + lw * 2048, lb + L_KVA + c * 8192 + lw * 2048);
#pragma unroll
  for (int c = 4; c < 7; ++c) dma_lin<2>(vl, wp + c * 8192 + lw * 2048, lb + L_KVB + (c - 4) * 8192 + lw * 2048);
  bar_only();
  STAMP(5);
#pragma unroll
  for (int c = 0; c < 7; ++c) dma_lin<1>(vl, wp + WP_Q + c * 4096 + lw * 1024, lb + L_Q + c * 4096 + lw * 1024);
  if (R.bwork) {
#pragma unroll 1
    for (int q = lw; q < 9; q += 4) {
      if (q < 7) touch(lane * XROW, (const char*)p.w2 + (size_t)(448 * R.tq + 64 * q) * XROW + cc * 128, lb);
      else touch(min(lane, q == 7 ? 63 : 47) * XROW, (const char*)p.wo + (size_t)(112 * R.tq + 64 * (q - 7)) * XROW + cc * 128, lb);
    }
    if (lw == 0) vmwait<10>(); else vmwait<9>();
  } else {
    vmwait<7>();
  }
  bar_only();
  if (j < 14) {
    LAS const char* src = (LAS const char*)(size_t)(lb + L_HS + lw * 8192) + vl;
    const u32 hb = OFF_H + (4 * i + lw) * H_TILE + 8 * j * 1024;
    u4v hv[8];
#pragma unroll
    for (int q = 0; q < 8; ++q) hv[q] = *(LAS const u4v*)(src + q * 1024);
#pragma unroll
    for (int q = 0; q < 8; ++q) st128(rs, vl, hb + q * 1024, hv[q]);
  }
  STAMP(14);
  vmwait<0>();
  if (j < 14 && lane == 0) __hip_atomic_store((GAS u64*)(p.ws + FL_H + ((i * 16 + j) * 4 + lw) * FLS), tag, __ATOMIC_RELAXED, __HIP_MEMORY_SCOPE_AGENT);
  cnt_arrive(lb + L_CNT + 8, lane);
  cnt_wait(lb + L_CNT + 4);
  STAMP(6);
  const char* w2p = (const char*)p.w2 + (size_t)(32 * lw) * XROW + cc * 128;
  auto issue_w = [&](int g) {
    if (g < 14) {
      dma4(vb0, vb1, vb2, vb3, w2p, lq + wslot(g));
      w2p += 128 * XROW;
    } else {
      dma4(vb0, vb1, vb2, vb3c, (const char*)p.wo + (size_t)(28 * (4 * (g - 14) + lw)) * XROW + cc * 128, lq + wslot(g));
    }
  };
  const char* hp = p.ws + OFF_H + (size_t)(2 * m64 + (lw >> 1)) * H_TILE + (lw & 1) * 4096;
  auto issue_h = [&](int g) {
    if (g == 14) hp = p.ws + OFF_O + (size_t)(2 * m64 + (lw >> 1)) * O_TILE + (lw & 1) * 4096;
    dma_lin<4>(vl, hp, lq + hslot(g));
    hp += 8192;
  };
  if (!R.bwork) return;
  issue_w(0);
  issue_w(1);
  issue_w(2);
  issue_w(3);
  flag_wait(p.ws, FL_H + (((m64 >> 1) * 16 + (lane >> 1)) * 4 + 2 * (m64 & 1) + (lane & 1)) * FLS, 28, tag, lane, 2);
  STAMP(7);
  cnt_wait(lb + L_CNT + 8);
  issue_h(0);
  touch(lane * 128, p.ws + OFF_H + (size_t)(2 * m64 + (lw >> 1)) * H_TILE + (16 * cc + 8 * (lw & 1)) * 1024, lb);
  issue_h(1);
  vmwait<5>(); bar_only();
  issue_h(2);
  vmwait<4>(); bar_only();
  issue_h(3);
  {
    const char* b = (const char*)p.x + (size_t)(64 * m64 + 16 * lw) * XROW + cc * 128;
    dma(vx0, b, lb + L_V + lw * 2048);
    dma(vx0 ^ 64, b + 8 * XROW, lb + L_V + lw * 2048 + 1024);
  }
  STAMP(8);
  vmwait<6>(); bar_only();
#pragma unroll 1
  for (int g = 2; g < 12; ++g) {
    issue_h(g + 2);
    issue_w(g + 2);
    vmwait<8>();
    bar_only();
  }
  issue_w(14);
  vmwait<4>(); bar_only();
  issue_w(15);
  dma4(vb0, vb1, vb2, vb3c, (const char*)p.wo + (size_t)(28 * (8 + lw)) * XROW + cc * 128, lb + L_PHI + lw * 4096);
  flag_wait(p.ws, FL_O + (((m64 >> 1) * 16 + (lane >> 1)) * 4 + 2 * (m64 & 1) + (lane & 1)) * FLS, 32, tag, lane, 3);
  STAMP(9);
  issue_h(14);
  issue_h(15);
  {
    const int line = min((4 * cc + lw) * 19 + min(lane, 18), 511);
    touch(line * 128 + (line >> 8) * 128, p.ws + OFF_O + (size_t)(2 * m64) * O_TILE, lb);
  }
  vmwait<5>(); bar_only();
  issue_h(16);
  issue_w(17);
  issue_h(17);
  vmwait<12>(); bar_only();
  vmwait<8>(); bar_only();
  vmwait<0>(); bar_only();
  STAMP(10);
  if (blockIdx.x == 0 && lw == 0 && lane == 0)
    __hip_atomic_store((GAS u32*)(p.ws + WS_EPOCH), E + 1, __ATOMIC_RELAXED, __HIP_MEMORY_SCOPE_AGENT);
  STAMP(11);
}

DI void exp_pair(const f16v& a, float c, f16v& ep, f16v& en) {
  float mx = fmx(a[0], a[1]), mn = fmn(a[0], a[1]);
#pragma unroll
  for (int r = 2; r < 16; r += 2) {
    mx = fmx(fmx(mx, a[r]), a[r + 1]);
    mn = fmn(fmn(mn, a[r]), a[r + 1]);
  }
  mx = xhalf_max(mx);
  mn = xhalf_min(mn);
  const f2v c2 = {c, c}, bp = {-mx * c, -mx * c}, bn = {mn * c, mn * c};
#pragma unroll
  for (int r = 0; r < 16; r += 2) {
    const f2v tp = __builtin_elementwise_fma(pair(a, r), c2, bp), tn = __builtin_elementwise_fma(pair(a, r), -c2, bn);
    ep[r] = __builtin_amdgcn_exp2f(tp[0]); ep[r + 1] = __builtin_amdgcn_exp2f(tp[1]);
    en[r] = __builtin_amdgcn_exp2f(tn[0]); en[r + 1] = __builtin_amdgcn_exp2f(tn[1]);
  }
}
DI float sum28(const f16v& e, int hh) {
  const f2v s0 = (pair(e, 0) + pair(e, 2)) + (pair(e, 4) + pair(e, 6)), s1 = pair(e, 8) + pair(e, 10), s2 = pair(e, 12) + pair(e, 14);
  const f2v t = s0 + s1;
  return xhalf_sum((t[0] + t[1]) + (hh ? 0.f : s2[0] + s2[1]));
}

DI void fold_load(const Params& p, u32 lb, int i, int head, int part, int lane, u4v (&a)[7]) {
  const int r16 = lane & 15, dg = lane >> 4;
  const int col = part == 0 ? XROW : (part == 1 ? 2 * XROW : 0);
  const char* src = (const char*)p.wqkv + (size_t)(32 * i + r16) * QROW + col + head * 56 + (dg == 3 ? 40 : dg * 16);
  a[0] = *(GAS const u4v*)src;
  a[1] = *(GAS const u4v*)(src + 16 * QROW);
  const char* wp = (const char*)p.wphi + head * 1568;
  const u32 dst = lb + L_PHI + part * 2048;
  dma(lane * 16, wp, dst);
  dma(min(lane, 33) * 16, wp + 1024, dst + 1024);
}
DI void fold_run(rsrc_t rs, LAS const char* lds, int i, int head, int part, int lane, const u4v (&a)[7]) {
  const int dg = lane >> 4, r16 = lane & 15;
  u4v bw[2];
  {
    const int q4 = r16 >> 2, pp = lane & 3;
    LAS const char* b = lds + L_PHI + part * 2048 + (8 * dg + q4) * 56 + pp * 8;
#pragma unroll
    for (int ft = 0; ft < 2; ++ft) {
      u4v v = __builtin_bit_cast(u4v, lds_tr2(b + ft * 32, b + ft * 32 + 4 * 56));
      if (dg == 3) { v[2] = 0u; v[3] = 0u; }
      if (part == 1) {
        const int t = 16 * ft + r16 - 8 * dg;
        const u32 one = (16 * ft + r16 < 28) ? ((t & 1) ? 0x3f800000u : 0x3f80u) : 0u;
#pragma unroll
        for (int q = 0; q < 4; ++q) v[q] = (t >> 1) == q ? one : 0u;
      }
      bw[ft] = v;
    }
  }
  const u32 base = OFF_WP + head * WP_HEAD + (part == 2 ? WP_Q + 2 * i * 1024 : 2 * i * 2048 + part * 1024);
  const u32 step = part == 2 ? 1024 : 2048;
  float cs = 0.f;
#pragma unroll
  for (int t = 0; t < 2; ++t) {
    u4v av = a[t];
    if (dg == 3) av = u4v{av[2], av[3], 0u, 0u};
    const f4v z = {0.f, 0.f, 0.f, 0.f};
    f4v d0 = mfma16(as_bf8(av), as_bf8(bw[0]), z), d1 = mfma16(as_bf8(av), as_bf8(bw[1]), z);
    if (part != 1) {
#pragma unroll
      for (int r = 0; r < 4; ++r) {
        const int v = __float_as_int(d1[r]);
        d1[r] = __int_as_float(__builtin_amdgcn_update_dpp(v, v, 0x15B, 0xf, 0x8, false));
      }
    }
    auto s0 = __builtin_amdgcn_permlane16_swap(pk2(d0[0], d0[1]), pk2(d1[0], d1[1]), false, false);
    auto s1 = __builtin_amdgcn_permlane16_swap(pk2(d0[2], d0[3]), pk2(d1[2], d1[3]), false, false);
    const u4v o = {s0[0], s1[0], s0[1], s1[1]};
    st128(rs, lane * 16, base + t * step, o);
    cs += ((bf_lo(o[0]) + bf_hi(o[0])) + (bf_lo(o[1]) + bf_hi(o[1]))) + ((bf_lo(o[2]) + bf_hi(o[2])) + (bf_lo(o[3]) + bf_hi(o[3])));
  }
  cs = xhalf_sum(cs);
  if (lane < 32) st32(rs, lane * 4, OFF_CSP + ((head * 3 + part) * 16 + i) * 128, __float_as_uint(cs));
}

constexpr int FD = 6, FNB = FD + 1;
template <int U>
DI void f_request(LAS const char* lds, u32 (&xa)[4], u32 (&wa)[4], bf8 (&x)[7][4], bf8 (&wf)[FNB]) {
  if constexpr (U < 112) {
    constexpr int c = U >> 4, s = (U >> 2) & 3, n = U & 3;
    if constexpr ((U & 31) == 0 && U > 0) {
#pragma unroll
      for (int q = 0; q < 4; ++q) {
        xa[q] ^= 65536u;
        wa[q] ^= 65536u;
        asm volatile("" : "+v"(xa[q]), "+v"(wa[q]));
      }
    }
    constexpr u32 xs = fslot(c) & 65535u, ws = xs + SLOT;
    if constexpr (n == 0) x[c][s] = lds_frag(lds + xs + xa[s]);
    wf[U % FNB] = lds_tr2(lds + ws + wa[n] + 4096 * s, lds + ws + wa[n] + 4096 * s + 1024);
  }
}
template <int C, int V>
DI void f_unit(LAS const char* lds, u32 (&xa)[4], u32 (&wa)[4], bf8 (&x)[7][4], bf8 (&wf)[FNB], f16v (&acc)[4]) {
  constexpr int U = 16 * C + V;
  if constexpr (V == 16 - FD && C < 6) bar_only();
  f_request<U + FD>(lds, xa, wa, x, wf);
  acc[V & 3] = mfma32(wf[U % FNB], x[C][V >> 2], acc[V & 3]);
  SCHED();
  if constexpr (V < 15) f_unit<C, V + 1>(lds, xa, wa, x, wf, acc);
}
template <int C>
DI void tick_f(LAS const char* lds, u32 (&xa)[4], u32 (&wa)[4], bf8 (&x)[7][4], bf8 (&wf)[FNB], f16v (&acc)[4]) {
  f_unit<C, 0>(lds, xa, wa, x, wf, acc);
}
template <int U>
DI void f_prime(LAS const char* lds, u32 (&xa)[4], u32 (&wa)[4], bf8 (&x)[7][4], bf8 (&wf)[FNB]) {
  f_request<U>(lds, xa, wa, x, wf);
  if constexpr (U + 1 < FD) f_prime<U + 1>(lds, xa, wa, x, wf);
}

DI void compute_main(const Params& p, LAS char* lds, const Role& R, int w, int lane, int wave) {
  const rsrc_t rs = __builtin_amdgcn_make_buffer_rsrc(p.ws, 0, WS_USED, 0x00020000);
  const int i = R.i, j = R.j;
  const int hh = lane >> 5, l31 = lane & 31;
  const bool ffn = j < 14;
  const bool fold = i < 14 && w < 3;
  const u32 E = *(CAS const u32*)(p.ws + WS_EPOCH);
  const u64 tag = tag64(E + 1);
  u32 xs7[7];
  const int stok = 128 * i + 8 * j + 2 * w + hh;
#pragma unroll
  for (int m = 0; m < 7; ++m) xs7[m] = *(GAS const u32*)((const char*)p.x + (size_t)stok * XROW + m * 128 + l31 * 4);
  auto stats = [&]() {
    f2v s2 = {0.f, 0.f};
#pragma unroll
    for (int m = 0; m < 7; ++m) s2 += f2v{bf_lo(xs7[m]), bf_hi(xs7[m])};
    const float mean = half_sum(s2[0] + s2[1]) * (1.f / D_);
    f2v q2 = {0.f, 0.f};
#pragma unroll
    for (int m = 0; m < 7; ++m) {
      const f2v a = f2v{bf_lo(xs7[m]), bf_hi(xs7[m])} - mean;
      q2 = __builtin_elementwise_fma(a, a, q2);
    }
    const float sigma = __builtin_amdgcn_sqrtf(half_sum(q2[0] + q2[1]) * (1.f / D_) + 1e-5f);
    if (l31 == 0) st128(rs, stok * 16, OFF_ST, u4v{__float_as_uint(mean), __float_as_uint(sigma), (u32)tag, (u32)(tag >> 32)});
  };
  u4v fc[7];
  if (fold) {
    fold_load(p, (u32)(size_t)lds, i, j, w, lane, fc);
  } else if (w == 3) {
    const char* src = (const char*)p.w1 + (size_t)(28 * i + (lane >> 4)) * W1ROW + R.jw * 256 + (lane & 15) * 16;
#pragma unroll
    for (int q = 0; q < 7; ++q) fc[q] = *(GAS const u4v*)(src + (size_t)q * 4 * W1ROW);
  }
  SCHED();
  bar_only();
  SCHED();
  STAMP(22);
  auto tasks = [&]() {
    __builtin_amdgcn_s_waitcnt(0x0F70);
    if (fold) {
      fold_run(rs, lds, i, j, w, lane, fc);
    } else if (w == 3) {
      float cs[8];
#pragma unroll
      for (int e = 0; e < 8; ++e) cs[e] = 0.f;
#pragma unroll
      for (int q = 0; q < 7; ++q)
#pragma unroll
        for (int e = 0; e < 4; ++e) { cs[2 * e] += bf_lo(fc[q][e]); cs[2 * e + 1] += bf_hi(fc[q][e]); }
#pragma unroll
      for (int e = 0; e < 8; ++e) {
        auto r = __builtin_amdgcn_permlane16_swap(__float_as_uint(cs[e]), __float_as_uint(cs[e]), false, false);
        cs[e] = xhalf_sum(__uint_as_float(r[0]) + __uint_as_float(r[1]));
      }
      if (ffn && lane < 32) {
        const int o = (lane >> 4) * 4;
        st128(rs, (lane & 15) * 32 + (lane >> 4) * 16, OFF_C1P + (j * 16 + i) * 512,
              u4v{__float_as_uint(o ? cs[4] : cs[0]), __float_as_uint(o ? cs[5] : cs[1]), __float_as_uint(o ? cs[6] : cs[2]), __float_as_uint(o ? cs[7] : cs[3])});
      }
    }
    stats();
  };
  u32 xa[4], wa[4];
  {
    const int row = 32 * w + l31;
    const u32 b = row * 128 + ((hh ^ ((row >> 1) & 7)) << 4);
#pragma unroll
    for (int s = 0; s < 4; ++s) xa[s] = b ^ (32 * s);
    const int q4 = (lane & 15) >> 2, pp = lane & 3, blk = (lane >> 4) & 1;
#pragma unroll
    for (int n = 0; n < 4; ++n) {
      const int nt = (n + w) & 3;
      wa[n] = (8 * hh + q4) * 256 + (4 * (nt ^ q4) + 2 * blk + (pp >> 1)) * 16 + 8 * (pp & 1);
    }
  }
  bf8 x[7][4], wf[FNB];
  f16v acc[4];
#pragma unroll
  for (int n = 0; n < 4; ++n)
#pragma unroll
    for (int r = 0; r < 16; ++r) acc[n][r] = 0.f;

  if (w == 0 && lane < 8) *(LAS volatile u32*)(lds + L_CNT + lane * 4) = 0u;
  bar();
  STAMP(2);
  tasks();
  STAMP(23);
  f_prime<0>(lds, xa, wa, x, wf);
  tick_f<0>(lds, xa, wa, x, wf, acc);
  STAMP(30);
  vmwait<1>();
  if (lane == 0) {
    if (fold) __hip_atomic_store((GAS u64*)(p.ws + FL_P0 + ((j * 3 + w) * 16 + i) * FLS), tag, __ATOMIC_RELAXED, __HIP_MEMORY_SCOPE_AGENT);
    if (ffn && w == 3) __hip_atomic_store((GAS u64*)(p.ws + FL_C1 + (j * 16 + i) * FLS), tag, __ATOMIC_RELAXED, __HIP_MEMORY_SCOPE_AGENT);
  }
  tick_f<1>(lds, xa, wa, x, wf, acc);
  STAMP(31);
  const int token = 128 * i + 32 * w + l31;
  u4v sg = ld128(rs, token * 16, OFF_ST);
  tick_f<2>(lds, xa, wa, x, wf, acc);
  STAMP(32);
  tick_f<3>(lds, xa, wa, x, wf, acc);
  STAMP(33);
  float mean = __uint_as_float(sg[0]), sigma = __uint_as_float(sg[1]);
  if (!__all(sg[2] == (u32)tag && sg[3] == (u32)(tag >> 32))) stat_wait(p.ws, rs, token, tag, lane, mean, sigma);
  tick_f<4>(lds, xa, wa, x, wf, acc);
  STAMP(34);
  bf8 xaug;
  {
    u32 mh, ml, sh, sl;
    split2(-mean, mh, ml);
    split2(sigma, sh, sl);
    u4v r = {mh | (ml << 16), mh | (sh << 16), sl, 0u};
    if (hh) r = u4v{0u, 0u, 0u, 0u};
    xaug = as_bf8(r);
  }
  const float rstd = __builtin_amdgcn_rcpf(sigma);
  tick_f<5>(lds, xa, wa, x, wf, acc);
  STAMP(35);
  tick_f<6>(lds, xa, wa, x, wf, acc);
  STAMP(4);
  bar();
  STAMP(6);
  const u32 vl = lane * 16;
  if (ffn) {
#pragma unroll
    for (int n = 0; n < 4; ++n) acc[n] = mfma32(lds_frag(lds + L_AUG + ((n + w) & 3) * 1024 + vl), xaug, acc[n]);
    LAS char* hb = lds + L_HS + w * 8192 + vl;
#pragma unroll
    for (int n = 0; n < 4; ++n) {
#pragma unroll
      for (int r = 0; r < 16; r += 2) {
        const f2v t = {fmx(acc[n][r], 0.f), fmx(acc[n][r + 1], 0.f)};
        const f2v u = t * t;
        acc[n][r] = u[0];
        acc[n][r + 1] = u[1];
      }
#pragma unroll
      for (int s = 0; s < 2; ++s) *(LAS bf8*)(hb + (2 * ((n + w) & 3) + s) * 1024) = frag8(acc[n], s);
    }
  }
  bar();
  STAMP(5);
  f16v ak, av;
#pragma unroll
  for (int r = 0; r < 16; ++r) { ak[r] = 0.f; av[r] = 0.f; }
  {
    u32 kva = vl + L_KVA;
    asm volatile("" : "+v"(kva));
    LAS const char* wa_ = lds + kva;
    LAS const char* wb_ = lds + vl + L_KVB;
    auto kvp = [&](int ks) -> LAS const char* { return ks < 16 ? wa_ + ks * 2048 : (ks < 28 ? wb_ + (ks - 16) * 2048 : wa_ + (L_AUG + 4 * 1024 - L_KVA)); };
    bf8 fk[4], fv[4];
#pragma unroll
    for (int ks = 0; ks < 3; ++ks) {
      fk[ks] = lds_frag(kvp(ks));
      fv[ks] = lds_frag(kvp(ks) + 1024);
    }
    SCHED();
#pragma unroll
    for (int ks = 0; ks < 29; ++ks) {
      if (ks + 3 < 29) {
        fk[(ks + 3) & 3] = lds_frag(kvp(ks + 3));
        fv[(ks + 3) & 3] = lds_frag(kvp(ks + 3) + 1024);
      }
      const bf8 xb = ks < 28 ? x[(ks < 28 ? ks : 0) >> 2][ks & 3] : xaug;
      ak = mfma32(fk[ks & 3], xb, ak);
      av = mfma32(fv[ks & 3], xb, av);
      __builtin_amdgcn_sched_group_barrier(0x8, 1, 0);
      __builtin_amdgcn_sched_group_barrier(0x100, 1, 0);
      __builtin_amdgcn_sched_group_barrier(0x8, 1, 0);
      __builtin_amdgcn_sched_group_barrier(0x100, 1, 0);
      SCHED();
    }
  }
  cnt_arrive((u32)(size_t)lds + L_CNT + 4, lane);
  STAMP(7);
  const float cexp = rstd * LOG2E;
  {
    f16v ep, en;
    exp_pair(ak, cexp, ep, en);
    const float ap = rstd * __builtin_amdgcn_rcpf(sum28(ep, hh)), an = rstd * __builtin_amdgcn_rcpf(sum28(en, hh));
    const int row = 32 * w + l31;
    LAS char* base = lds + row * 64 + 8 * hh;
    const int key = (l31 >> 1) & 3;
#pragma unroll
    for (int q = 0; q < 4; ++q) {
      const u32 o = (q ^ key) << 4;
      *(LAS u2v*)(base + L_PHI + o) = u2v{pk2v(pair(ep, 4 * q) * ap), pk2v(pair(ep, 4 * q + 2) * ap)};
      *(LAS u2v*)(base + L_PHI + 8192 + o) = u2v{pk2v(pair(en, 4 * q) * an), pk2v(pair(en, 4 * q + 2) * an)};
      *(LAS u2v*)(base + L_V + o) = u2v{pk2(av[4 * q], av[4 * q + 1]), pk2(av[4 * q + 2], av[4 * q + 3])};
    }
  }
  STAMP(8);
  cnt_arrive((u32)(size_t)lds + L_CNT, lane);
  if (w < 2) {
    cnt_wait((u32)(size_t)lds + L_CNT);
    STAMP(9);
    const int q4 = (lane & 15) >> 2, pp = lane & 3, blk = (lane >> 4) & 1;
    u32 ta[2];
#pragma unroll
    for (int t = 0; t < 2; ++t) {
      const int row = 8 * hh + 4 * t + q4;
      ta[t] = row * 64 + (((2 * blk + (pp >> 1)) ^ ((row >> 1) & 3)) << 4) + 8 * (pp & 1);
    }
    f16v kv;
#pragma unroll
    for (int r = 0; r < 16; ++r) kv[r] = 0.f;
    LAS const char* pa = lds + L_PHI + w * 8192;
    LAS const char* pb = lds + L_V;
#pragma unroll
    for (int ks = 0; ks < 8; ++ks)
      kv = mfma32(lds_tr2(pa + ta[0] + ks * 1024, pa + ta[1] + ks * 1024), lds_tr2(pb + ta[0] + ks * 1024, pb + ta[1] + ks * 1024), kv);
    const u32 o = OFF_KVP + (j * 2 + w) * KVP_HS + i * 2048;
    st128(rs, vl, o, __builtin_bit_cast(u4v, frag8(kv, 0)));
    st128(rs, vl, o + 1024, __builtin_bit_cast(u4v, frag8(kv, 1)));
  }
  STAMP(18);
  cnt_wait((u32)(size_t)lds + L_CNT + 8);
  f16v aq;
#pragma unroll
  for (int r = 0; r < 16; ++r) aq[r] = 0.f;
  {
    LAS const char* wb = lds + L_Q + vl;
    bf8 fq[6];
#pragma unroll
    for (int ks = 0; ks < 5; ++ks) fq[ks] = lds_frag(wb + ks * 1024);
    SCHED();
#pragma unroll
    for (int ks = 0; ks < 29; ++ks) {
      if (ks + 5 < 28) fq[(ks + 5) % 6] = lds_frag(wb + (ks + 5) * 1024);
      else if (ks + 5 == 28) fq[(ks + 5) % 6] = lds_frag(lds + L_AUG + 6 * 1024 + vl);
      aq = mfma32(fq[ks % 6], ks < 28 ? x[(ks < 28 ? ks : 0) >> 2][ks & 3] : xaug, aq);
      SCHED();
      if (ks == 15 && w < 2) flag_set(p.ws, FL_KV + ((j * 2 + w) * 16 + i) * FLS, tag, lane);
    }
  }
  STAMP(10);
  bf8 eq[2][2];
  auto qepi = [&]() {
    f16v ep, en;
    exp_pair(aq, cexp, ep, en);
    eq[0][0] = frag8(ep, 0); eq[0][1] = frag8(ep, 1);
    eq[1][0] = frag8(en, 0); eq[1][1] = frag8(en, 1);
  };
  const int m64 = R.m64, cc = R.cc;
  const int bm = w & 1, bn = w >> 1;
  u32 ha, wb;
  {
    const int q4 = (lane & 15) >> 2, pp = lane & 3, blk = (lane >> 4) & 1;
    ha = bm * 8192 + bn * 4096 + vl;
    wb = bn * 8192 + (4 * hh + q4) * 128 + (4 * (q4 >> 1) + 2 * blk + (pp >> 1)) * 16 + 8 * (pp & 1);
  }
  const u4v sg2 = ld128(rs, (64 * m64 + 32 * bm + l31) * 16, OFF_ST);
  const u32 b2 = *((const GAS u16*)p.b2 + cc * 64 + bn * 32 + l31);
  f16v a0, a1;
#pragma unroll
  for (int r = 0; r < 16; ++r) { a0[r] = 0.f; a1[r] = 0.f; }
  bf8 fh[4], f0[4], f1[4];
  u32 hp = 0, wp = 0;
  auto point = [&](int g) { hp = hslot(g) + ha; wp = (g == 16 ? L_PHI : wslot(g)) + wb; };
  auto req = [&](int ks) {
    fh[ks] = lds_frag(lds + hp + ks * 1024);
    f0[ks] = lds_tr2(lds + wp + ks * 2048, lds + wp + ks * 2048 + 1024);
    f1[ks] = lds_tr2(lds + (wp ^ 64) + ks * 2048, lds + (wp ^ 64) + ks * 2048 + 1024);
  };
  auto mm = [&](int ks) {
    a0 = mfma32(f0[ks], fh[ks], a0);
    a1 = mfma32(f1[ks], fh[ks], a1);
    __builtin_amdgcn_sched_group_barrier(0x8, 1, 0);
    __builtin_amdgcn_sched_group_barrier(0x100, 3, 0);
    __builtin_amdgcn_sched_group_barrier(0x8, 1, 0);
    __builtin_amdgcn_sched_group_barrier(0x100, 2, 0);
    SCHED();
  };
  auto tick = [&](int g) {
    req(3); mm(0);
    bar_only();
    point(g + 1);
    req(0); mm(1);
    req(1); mm(2);
    req(2); mm(3);
  };
  const u32 krflag = FL_KR + (j * 8 + min(lane, 7)) * FLS;
  int g = 0;
  u4v kt[4];
  auto oload = [&]() {
#pragma unroll
    for (int q = 0; q < 4; ++q) kt[q] = ld128(rs, vl, OFF_KVT + (j * 4 + q) * 1024);
  };
  auto ostore = [&]() {
    f16v np, nn;
#pragma unroll
    for (int r = 0; r < 16; ++r) { np[r] = 0.f; nn[r] = 0.f; }
    np = mfma32(as_bf8(kt[0]), eq[0][0], np);
    nn = mfma32(as_bf8(kt[2]), eq[1][0], nn);
    np = mfma32(as_bf8(kt[1]), eq[0][1], np);
    nn = mfma32(as_bf8(kt[3]), eq[1][1], nn);
    const float ia = __builtin_amdgcn_rcpf(xhalf_hi(np[13])), ib = __builtin_amdgcn_rcpf(xhalf_hi(nn[13]));
    const float den = xhalf_hi(np[12]) * ia + xhalf_hi(nn[12]) * ib + 1e-6f;
    const float id = __builtin_amdgcn_rcpf(den), fa = ia * id, fb = ib * id;
    u32 ov[8];
#pragma unroll
    for (int r = 0; r < 16; r += 2) ov[r >> 1] = pk2v(pair(np, r) * fa + pair(nn, r) * fb);
    if (hh) { ov[6] = 0u; ov[7] = 0u; }
    const u32 ob = OFF_O + (4 * i + w) * O_TILE + 2 * j * 1024;
    st128(rs, vl, ob, u4v{ov[0], ov[1], ov[2], ov[3]});
    st128(rs, vl, ob + 1024, u4v{ov[4], ov[5], ov[6], ov[7]});
  };
  const u32 oflag = FL_O + ((i * 16 + j) * 4 + w) * FLS;
  if (R.bwork) {
    bar();
    point(0);
    req(0); req(1); req(2);
    SCHED();
    tick(0);
    tick(1);
    g = 2;
    qepi();
    SCHED();
    u64 kr = 0;
    bool ready = false;
#pragma unroll 1
    while (g < 10) {
      tick(g);
      ++g;
      if (__all(kr == tag)) { ready = true; break; }
      if (g >= 4) kr = __hip_atomic_load((GAS u64*)(p.ws + krflag), __ATOMIC_RELAXED, __HIP_MEMORY_SCOPE_AGENT);
    }
    if (!ready) flag_wait(p.ws, krflag, 8, tag, lane, 6);
    STAMP(14);
    oload();
    tick(g); ++g;
    ostore();
    tick(g); ++g;
    flag_set(p.ws, oflag, tag, lane);
    STAMP(15);
  } else {
    const int hs = R.rho, sk = w & 1, hf = w >> 1;
    flag_wait(p.ws, FL_KV + (hs * 16 + lane) * FLS, 16, tag, lane, 5);
    STAMP(12);
    const u32 o = OFF_KVP + hs * KVP_HS + sk * 1024 + hf * 512;
    u4v t[8];
#pragma unroll
    for (int q = 0; q < 8; ++q) t[q] = ld128(rs, hh * 8 * 2048 + l31 * 16, o + q * 2048);
    f2v sm[4];
#pragma unroll
    for (int e = 0; e < 4; ++e) sm[e] = f2v{bf_lo(t[0][e]), bf_hi(t[0][e])};
#pragma unroll
    for (int q = 1; q < 8; ++q)
#pragma unroll
      for (int e = 0; e < 4; ++e) sm[e] += f2v{bf_lo(t[q][e]), bf_hi(t[q][e])};
    u4v r;
#pragma unroll
    for (int e = 0; e < 4; ++e) r[e] = pk2(xhalf_sum(sm[e][0]), xhalf_sum(sm[e][1]));
    if (l31 == 29) r = u4v{0x3f803f80u, 0x3f803f80u, 0x3f803f80u, 0x3f803f80u};
    if (l31 >= 30) r = u4v{0u, 0u, 0u, 0u};
    if (sk == 1 && hf == 1) { r[2] = 0u; r[3] = 0u; }
    if (lane < 32) st128(rs, l31 * 16, OFF_KVT + (hs * 2 + sk) * 1024 + hf * 512, r);
    flag_set(p.ws, FL_KR + ((hs * 2 + sk) * 2 + hf) * FLS, tag, lane);
    STAMP(13);
    qepi();
    flag_wait(p.ws, krflag, 8, tag, lane, 6);
    STAMP(14);
    oload();
    ostore();
    flag_set(p.ws, oflag, tag, lane);
    STAMP(15);
    return;
  }
#pragma unroll 1
  for (; g < 13; ++g) tick(g);
  req(3); mm(0); mm(1); mm(2); mm(3);
  STAMP(24);
  float sil[16];
  u2v xr[4];
  {
    const u32 cb = (u32)(size_t)lds + L_CNT;
    cnt_arrive(cb + 12, lane);
    cnt_wait(cb + 12);
    LAS char* dst = lds + 6 * SLOT + (bm * 2 + (1 - bn)) * 4096 + vl;
    LAS const char* src = lds + 6 * SLOT + (bm * 2 + bn) * 4096 + vl;
#pragma unroll
    for (int q = 0; q < 4; ++q)
      *(LAS f4v*)(dst + q * 1024) = bn ? f4v{a0[4 * q], a0[4 * q + 1], a0[4 * q + 2], a0[4 * q + 3]} : f4v{a1[4 * q], a1[4 * q + 1], a1[4 * q + 2], a1[4 * q + 3]};
    cnt_arrive(cb + 16, lane);
    float m2 = __uint_as_float(sg2[0]), s2 = __uint_as_float(sg2[1]);
    if (!__all(sg2[2] == (u32)tag && sg2[3] == (u32)(tag >> 32))) stat_wait(p.ws, rs, 64 * m64 + 32 * bm + l31, tag, lane, m2, s2);
    const float var = s2 * s2;
    u32 vh, vlo;
    split2(var, vh, vlo);
    u4v xa2 = {vh | (vlo << 16), 0u, 0u, 0u};
    u4v wa3 = {b2 | (b2 << 16), 0u, 0u, 0u};
    if (hh) { xa2[0] = 0u; wa3[0] = 0u; }
    f16v ag = bn ? a1 : a0;
    ag = mfma32(as_bf8(wa3), as_bf8(xa2), ag);
    const float r2 = __builtin_amdgcn_rcpf(var);
    {
      const int row = 32 * bm + l31, key = (row >> 1) & 7;
      LAS const char* base = lds + L_V + row * 128 + 8 * hh;
#pragma unroll
      for (int q = 0; q < 4; ++q) xr[q] = *(LAS const u2v*)(base + (((4 * bn + q) ^ key) << 4));
    }
    cnt_wait(cb + 16);
#pragma unroll
    for (int q = 0; q < 4; ++q) {
      const f4v gv = *(LAS const f4v*)(src + q * 1024);
#pragma unroll
      for (int e = 0; e < 4; ++e) {
        const float gp = (ag[4 * q + e] + gv[e]) * r2;
        sil[4 * q + e] = gp * __builtin_amdgcn_rcpf(1.f + __builtin_amdgcn_exp2f(-gp * LOG2E));
      }
    }
  }
  f16v as;
#pragma unroll
  for (int r = 0; r < 16; ++r) as[r] = 0.f;
  {
    const int q4 = (lane & 15) >> 2, pp = lane & 3, blk = (lane >> 4) & 1;
    const u32 hs = bm * 8192 + vl, ws = (4 * hh + q4) * 128 + (4 * (bn ^ (q4 >> 1)) + 2 * blk + (pp >> 1)) * 16 + 8 * (pp & 1);
    bf8 sh[2][8], sw[2][8];
    auto rd = [&](int set, u32 hslot_, u32 wslot_) {
      LAS const char* hp_ = lds + hslot_ + hs;
      LAS const char* wp_ = lds + wslot_ + ws;
#pragma unroll
      for (int ks = 0; ks < 8; ++ks) {
        sh[set][ks] = lds_frag(hp_ + ks * 1024);
        sw[set][ks] = lds_tr2(wp_ + ks * 2048, wp_ + ks * 2048 + 1024);
      }
    };
    auto ms = [&](int set) {
#pragma unroll
      for (int ks = 0; ks < 8; ++ks) as = mfma32(sw[set][ks], sh[set][ks], as);
    };
    auto mix = [&]() {
#pragma unroll
      for (int ks = 0; ks < 8; ++ks) {
        __builtin_amdgcn_sched_group_barrier(0x8, 1, 0);
        __builtin_amdgcn_sched_group_barrier(0x100, 3, 0);
      }
      SCHED();
    };
    bar_only();
    rd(0, 2 * SLOT, 0 * SLOT);
    SCHED();
    bar_only();
    rd(1, 3 * SLOT, 1 * SLOT); ms(0); mix();
    bar_only();
    rd(0, 4 * SLOT, L_PHI); ms(1); mix();
    bar_only();
    rd(1, 5 * SLOT, 7 * SLOT); ms(0); mix();
    ms(1);
  }
  STAMP(16);
  {
    u32 ov[4][2];
#pragma unroll
    for (int q = 0; q < 4; ++q) {
      const float o0 = __builtin_fmaf(as[4 * q], sil[4 * q], bf_lo(xr[q][0])), o1 = __builtin_fmaf(as[4 * q + 1], sil[4 * q + 1], bf_hi(xr[q][0]));
      const float o2 = __builtin_fmaf(as[4 * q + 2], sil[4 * q + 2], bf_lo(xr[q][1])), o3 = __builtin_fmaf(as[4 * q + 3], sil[4 * q + 3], bf_hi(xr[q][1]));
      ov[q][0] = pk2(o0, o1);
      ov[q][1] = pk2(o2, o3);
    }
    char* orow = (char*)p.out + (size_t)(64 * m64 + 32 * bm + l31) * XROW + cc * 128 + bn * 64 + hh * 16;
#pragma unroll
    for (int q = 0; q < 4; q += 2) {
      auto r0 = __builtin_amdgcn_permlane32_swap(ov[q][0], ov[q + 1][0], false, false);
      auto r1 = __builtin_amdgcn_permlane32_swap(ov[q][1], ov[q + 1][1], false, false);
      *(GAS u4v*)(orow + q * 16) = u4v{r0[0], r1[0], r0[1], r1[1]};
    }
  }
  STAMP(17);
  DIAG_WAIT();
  STAMPC(63);
}

__global__ __launch_bounds__(512, 2) void fused_kernel(Params p) {
  extern __shared__ __attribute__((aligned(16))) char smem[];
  LAS char* lds = (LAS char*)smem;
  const int lane = threadIdx.x & 63;
  const int wave = __builtin_amdgcn_readfirstlane(threadIdx.x >> 6);
  const int b = blockIdx.x, chi = b & 7, sidx = b >> 3;
  Role R;
  R.i = 8 * (chi & 1) + (sidx & 7);
  R.j = 4 * (chi >> 1) + (sidx >> 3);
  R.jw = min(R.j, 13);
  R.bwork = sidx < 28;
  R.tq = R.bwork ? sidx / 7 : 0;
  R.cc = R.bwork ? sidx % 7 : 0;
  R.m64 = 4 * chi + R.tq;
  R.rho = 4 * chi + (sidx - 28);
  R.sidx = sidx;
  DIAG_INIT();
  STAMP(0);
  DIAG_WAIT();
  STAMPC(62);
  DIAG_WAIT();
  if (wave >= 4) loader_main(p, (u32)(size_t)lds, R, wave - 4, lane, wave);
  else compute_main(p, lds, R, wave, lane, wave);
  DIAG_DUMP();
}

extern "C" void kernel_launch(void* const* d_in, const int* in_sizes, int n_in, void* d_out, int out_size, void* d_ws,
                              size_t ws_size, hipStream_t stream) {
  Params p;
  p.x = (const u16*)d_in[0];
  p.wqkv = (const u16*)d_in[1];
  p.wphi = (const u16*)d_in[2];
  p.wo = (const u16*)d_in[3];
  p.w1 = (const u16*)d_in[5];
  p.b1 = (const u16*)d_in[6];
  p.w2 = (const u16*)d_in[7];
  p.b2 = (const u16*)d_in[8];
  p.out = (u16*)d_out;
  p.ws = (char*)d_ws;
  (void)hipFuncSetAttribute((const void*)fused_kernel, hipFuncAttributeMaxDynamicSharedMemorySize, LDS_BYTES);
  fused_kernel<<<256, 512, LDS_BYTES, stream>>>(p);
}
```
